```python
import torch

def _softmax(x, axis=-1):
    return torch.softmax(x, dim=axis)

def _gelu(x):
    return torch.nn.functional.gelu(x, approximate='tanh')

def _silu(x):
    return torch.nn.functional.silu(x)

def _sigmoid(x):
    return torch.sigmoid(x)

def _layernorm(x, axis=-1, eps=1e-05):
    m = x.mean(dim=axis, keepdim=True)
    v = x.var(dim=axis, keepdim=True, unbiased=False)
    return (x - m) / torch.sqrt(v + eps)

def _scatter_add(tgt, idx, src, axis=0):
    return tgt.clone().index_add_(axis, idx, src)

def _segment_sum(src, seg, n):
    out = torch.zeros((n,) + src.shape[1:], dtype=src.dtype)
    return out.index_add_(0, seg, src)

def _pad(x, pw):
    flat = tuple((v for pair in reversed(pw) for v in pair))
    return torch.nn.functional.pad(x, flat)
kernel_name = 'adaLN_cond__gqa_block_cached__mixer_channel_mlp__decode'

def setup_inputs():

    _w = lambda a: (lambda t: t.to(torch.bfloat16) if t.is_floating_point() else t)(torch.as_tensor(a))

    def _A_setup():
        B, Sq, D = (2, 1, 1024)
        Hq, Hkv, Sk, Dh = (32, 4, 32768, 128)
        assert Hq % Hkv == 0, (Hq, Hkv)
        return {'x': _w((lambda *s: torch.randn(*s, dtype=torch.bfloat16) * 0.1)(B, Sq, D)), 'k_cache': _w((lambda *s: torch.randn(*s, dtype=torch.bfloat16) * 1.6)(B, Hkv, Sk, Dh)), 'v_cache': _w((lambda *s: torch.randn(*s, dtype=torch.bfloat16) * 0.8)(B, Hkv, Sk, Dh)), 'wq': _w((lambda *s: torch.randn(*s, dtype=torch.bfloat16) * 1.6)(D, Hq * Dh) * D ** (-0.5)), 'wo': _w((lambda *s: torch.randn(*s, dtype=torch.bfloat16) * 0.8)(Hq * Dh, D) * (Hq * Dh) ** (-0.5))}
    _A_in = _A_setup()

    def _B_setup():
        B, S, D, F = (2, 1, 1024, 3584)
        return {'x': _w((lambda *s: torch.randn(*s, dtype=torch.bfloat16) * 0.1)(B, S, D)), 'w1': _w((lambda *s: torch.randn(*s, dtype=torch.bfloat16) * 3.2)(D, F) * D ** (-0.5)), 'b1': _w((lambda *s: torch.randn(*s, dtype=torch.bfloat16) * 3.2)(F)), 'w2': _w((lambda *s: torch.randn(*s, dtype=torch.bfloat16) * 3.2)(F, D) * F ** (-0.5)), 'b2': _w((lambda *s: torch.randn(*s, dtype=torch.bfloat16) * 3.2)(D))}
    _B_in = _B_setup()
    return {'A_x': _A_in['x'], 'A_k_cache': _A_in['k_cache'], 'A_v_cache': _A_in['v_cache'], 'A_wq': _A_in['wq'], 'A_wo': _A_in['wo'], 'B_x': _B_in['x'], 'B_w1': _B_in['w1'], 'B_b1': _B_in['b1'], 'B_w2': _B_in['w2'], 'B_b2': _B_in['b2'], 'c': _w((lambda *s: torch.randn(*s, dtype=torch.bfloat16) * 0.2)(2, 1024) * 8.0), 'w_mod': _w((lambda *s: torch.randn(*s, dtype=torch.bfloat16) * 0.2)(1024, 6 * 1024) * 1024 ** (-0.5) * 8.0)}

def reference(A_x, A_k_cache, A_v_cache, A_wq, A_wo, B_x, B_w1, B_b1, B_w2, B_b2, c, w_mod):

    def _A_ref(x, k_cache, v_cache, wq, wo):
        B, Sq, D = x.shape
        Hq, Dh = (32, 128)
        Hkv = k_cache.shape[1]
        rep = Hq // Hkv
        q = (lambda x, *d: x.permute(*d))((x @ wq).reshape(B, Sq, Hq, Dh), 0, 2, 1, 3)
        k = (lambda x, n, axis: torch.repeat_interleave(x, n, dim=axis))(k_cache, rep, axis=1)
        v = (lambda x, n, axis: torch.repeat_interleave(x, n, dim=axis))(v_cache, rep, axis=1)
        scores = torch.einsum('bhqd,bhkd->bhqk', q, k) * Dh ** (-0.5)
        p = _softmax(scores, axis=-1)
        o = torch.einsum('bhqk,bhkd->bhqd', p, v)
        return (lambda x, *d: x.permute(*d))(o, 0, 2, 1, 3).reshape(B, Sq, Hq * Dh) @ wo

    def _B_ref(x, w1, b1, w2, b2):
        y = _layernorm(x, axis=-1)
        h = _gelu(torch.einsum('bsd,df->bsf', y, w1) + b1)
        return torch.einsum('bsf,fd->bsd', h, w2) + b2
    x = A_x
    mod = (_silu(c) @ w_mod).reshape(c.shape[0], 6, 1, -1)
    sa, ca, ga, sb, cb, gb = (mod[:, 0].clone(), mod[:, 1].clone(), mod[:, 2].clone(), mod[:, 3].clone(), mod[:, 4].clone(), mod[:, 5].clone())
    h = _layernorm(x, axis=-1) * (1 + ca) + sa
    x = x + ga * _A_ref(h, A_k_cache, A_v_cache, A_wq, A_wo)
    h = _layernorm(x, axis=-1) * (1 + cb) + sb
    x = x + gb * _B_ref(h, B_w1, B_b1, B_w2, B_b2)
    return x
```

```cpp
#include <hip/hip_runtime.h>
#include <hip/hip_bf16.h>
typedef unsigned v4u __attribute__((ext_vector_type(4)));
typedef unsigned short u16;
constexpr int D = 1024, HQ = 32, HKV = 4, SK = 32768, DH = 128, F = 3584, NSPLIT = 32;
__device__ __forceinline__ float bf2f(unsigned b) { return __builtin_bit_cast(float, b << 16); }
__device__ __forceinline__ u16 f2bf(float f) { unsigned u = __builtin_bit_cast(unsigned, f); u += 0x7FFFu + ((u >> 16) & 1u); return (u16)(u >> 16); }

template <int K>
__device__ __forceinline__ void gemv128(const float* sin, const u16* W, int ldw, int n0, float* red, float& r0, float& r1) {
  const int tid = threadIdx.x, c = tid & 15, g = tid >> 4;
  float a0[8] = {0}, a1[8] = {0};
  for (int k = g; k < K; k += 16) {
    v4u w = *(const v4u*)(W + (size_t)k * ldw + n0 + 8 * c);
    float y0 = sin[k], y1 = sin[K + k];
#pragma unroll
    for (int j = 0; j < 4; ++j) {
      float lo = bf2f(w[j] & 0xffffu), hi = bf2f(w[j] >> 16);
      a0[2 * j] += y0 * lo; a0[2 * j + 1] += y0 * hi; a1[2 * j] += y1 * lo; a1[2 * j + 1] += y1 * hi;
    }
  }
#pragma unroll
  for (int j = 0; j < 8; ++j) { red[(g * 2 + 0) * 128 + 8 * c + j] = a0[j]; red[(g * 2 + 1) * 128 + 8 * c + j] = a1[j]; }
  __syncthreads();
  r0 = 0; r1 = 0;
  const int b = tid >> 7, n = tid & 127;
  float s = 0;
  for (int gg = 0; gg < 16; ++gg) s += red[(gg * 2 + b) * 128 + n];
  r0 = s;
}
__device__ __forceinline__ float block_sum(float v, float* sc) {
  for (int o = 32; o > 0; o >>= 1) v += __shfl_xor(v, o);
  __syncthreads();
  if ((threadIdx.x & 63) == 0) sc[threadIdx.x >> 6] = v;
  __syncthreads();
  return sc[0] + sc[1] + sc[2] + sc[3];
}
__device__ __forceinline__ void ln2(float* x, float* sc) {
  for (int b = 0; b < 2; ++b) {
    float s = 0; for (int i = threadIdx.x; i < D; i += 256) s += x[b * D + i];
    float m = block_sum(s, sc) / D;
    float v = 0; for (int i = threadIdx.x; i < D; i += 256) { float d = x[b * D + i] - m; v += d * d; }
    float var = block_sum(v, sc) / D;
    float r = 1.0f / sqrtf(var + 1e-5f);
    for (int i = threadIdx.x; i < D; i += 256) x[b * D + i] = (x[b * D + i] - m) * r;
    __syncthreads();
  }
}
__global__ void __launch_bounds__(256) k_mod(const u16* c, const u16* wmod, float* mod) {
  __shared__ float sin[2 * D]; __shared__ float red[16 * 2 * 128];
  for (int i = threadIdx.x; i < 2 * D; i += 256) { float v = bf2f(c[i]); sin[i] = v / (1.0f + __expf(-v)); }
  __syncthreads();
  float r0, r1; gemv128<D>(sin, wmod, 6 * D, blockIdx.x * 128, red, r0, r1);
  mod[(threadIdx.x >> 7) * 6 * D + blockIdx.x * 128 + (threadIdx.x & 127)] = r0;
}
__global__ void __launch_bounds__(256) k_q(const u16* x, const float* mod, const u16* wq, float* q) {
  __shared__ float sin[2 * D]; __shared__ float red[16 * 2 * 128]; __shared__ float sc[4];
  for (int i = threadIdx.x; i < 2 * D; i += 256) sin[i] = bf2f(x[i]);
  __syncthreads();
  ln2(sin, sc);
  for (int i = threadIdx.x; i < 2 * D; i += 256) { int b = i >> 10, k = i & 1023; sin[i] = sin[i] * (1.0f + mod[b * 6 * D + D + k]) + mod[b * 6 * D + k]; }
  __syncthreads();
  float r0, r1; gemv128<D>(sin, wq, HQ * DH, blockIdx.x * 128, red, r0, r1);
  q[(threadIdx.x >> 7) * HQ * DH + blockIdx.x * 128 + (threadIdx.x & 127)] = r0;
}
__global__ void __launch_bounds__(256) k_attn(const float* q, const u16* K, const u16* V, float* po, float* pml) {
  __shared__ float sq[8 * 128]; __shared__ float ss[8 * 1024]; __shared__ float red[16 * 8 * 128 / 4]; __shared__ float sm[8], sl[8];
  const int s = blockIdx.x, kvh = blockIdx.y, b = blockIdx.z, tid = threadIdx.x;
  for (int i = tid; i < 1024; i += 256) sq[i] = q[b * HQ * DH + kvh * 1024 + i] * 0.08838834764831845f;
  __syncthreads();
  const u16* Kp = K + ((size_t)(b * HKV + kvh) * SK + s * 1024) * DH;
  const u16* Vp = V + ((size_t)(b * HKV + kvh) * SK + s * 1024) * DH;
  const int c = tid & 15, g = tid >> 4;
  float qr[8][8];
#pragma unroll
  for (int h = 0; h < 8; ++h)
#pragma unroll
    for (int j = 0; j < 8; ++j) qr[h][j] = sq[h * 128 + 8 * c + j];
  for (int key = g; key < 1024; key += 16) {
    v4u w = __builtin_nontemporal_load((const v4u*)(Kp + (size_t)key * DH + 8 * c));
    float kf[8];
#pragma unroll
    for (int j = 0; j < 4; ++j) { kf[2 * j] = bf2f(w[j] & 0xffffu); kf[2 * j + 1] = bf2f(w[j] >> 16); }
#pragma unroll
    for (int h = 0; h < 8; ++h) {
      float d = 0;
#pragma unroll
      for (int j = 0; j < 8; ++j) d += qr[h][j] * kf[j];
      d += __shfl_xor(d, 1); d += __shfl_xor(d, 2); d += __shfl_xor(d, 4); d += __shfl_xor(d, 8);
      if (c == h) ss[h * 1024 + key] = d;
    }
  }
  __syncthreads();
  {
    const int h = tid >> 5, l = tid & 31;
    float m = -INFINITY;
    for (int k = l; k < 1024; k += 32) m = fmaxf(m, ss[h * 1024 + k]);
    for (int o = 16; o > 0; o >>= 1) m = fmaxf(m, __shfl_xor(m, o));
    float sum = 0;
    for (int k = l; k < 1024; k += 32) { float p = __expf(ss[h * 1024 + k] - m); ss[h * 1024 + k] = p; sum += p; }
    for (int o = 16; o > 0; o >>= 1) sum += __shfl_xor(sum, o);
    if (l == 0) { sm[h] = m; sl[h] = sum; }
  }
  __syncthreads();
  float acc[8][8];
#pragma unroll
  for (int h = 0; h < 8; ++h)
#pragma unroll
    for (int j = 0; j < 8; ++j) acc[h][j] = 0;
  for (int key = g; key < 1024; key += 16) {
    v4u w = __builtin_nontemporal_load((const v4u*)(Vp + (size_t)key * DH + 8 * c));
    float vf[8];
#pragma unroll
    for (int j = 0; j < 4; ++j) { vf[2 * j] = bf2f(w[j] & 0xffffu); vf[2 * j + 1] = bf2f(w[j] >> 16); }
#pragma unroll
    for (int h = 0; h < 8; ++h) { float p = ss[h * 1024 + key];
#pragma unroll
      for (int j = 0; j < 8; ++j) acc[h][j] += p * vf[j]; }
  }
#pragma unroll
  for (int h = 0; h < 8; ++h)
#pragma unroll
    for (int j = 0; j < 8; ++j) { float v = acc[h][j]; v += __shfl_xor(v, 16); v += __shfl_xor(v, 32); acc[h][j] = v; }
  const int w = tid >> 6;
  if ((tid & 63) < 16) {
#pragma unroll
    for (int h = 0; h < 8; ++h)
#pragma unroll
      for (int j = 0; j < 8; ++j) red[(w * 8 + h) * 128 + 8 * c + j] = acc[h][j];
  }
  __syncthreads();
  for (int i = tid; i < 1024; i += 256) {
    float v = red[i] + red[1024 + i] + red[2048 + i] + red[3072 + i];
    const int h = i >> 7, d = i & 127;
    po[(((size_t)b * HQ + kvh * 8 + h) * NSPLIT + s) * DH + d] = v;
  }
  if (tid < 8) { float* p = pml + (((size_t)b * HQ + kvh * 8 + tid) * NSPLIT + s) * 2; p[0] = sm[tid]; p[1] = sl[tid]; }
}
__global__ void __launch_bounds__(128) k_comb(const float* po, const float* pml, float* o) {
  const int row = blockIdx.x, d = threadIdx.x;
  float M = -INFINITY;
  for (int s = 0; s < NSPLIT; ++s) M = fmaxf(M, pml[(row * NSPLIT + s) * 2]);
  float L = 0, a = 0;
  for (int s = 0; s < NSPLIT; ++s) { float w = __expf(pml[(row * NSPLIT + s) * 2] - M); L += w * pml[(row * NSPLIT + s) * 2 + 1]; a += w * po[((size_t)row * NSPLIT + s) * DH + d]; }
  o[row * DH + d] = a / L;
}
template <int KS>
__global__ void __launch_bounds__(256) k_gemv_part(const float* in, int Ktot, const u16* W, int ldw, float* part) {
  __shared__ float sin[2 * KS]; __shared__ float red[16 * 2 * 128];
  const int k0 = blockIdx.y * KS;
  for (int i = threadIdx.x; i < 2 * KS; i += 256) sin[i] = in[(i / KS) * Ktot + k0 + (i % KS)];
  __syncthreads();
  float r0, r1; gemv128<KS>(sin, W + (size_t)k0 * ldw, ldw, blockIdx.x * 128, red, r0, r1);
  part[((size_t)blockIdx.y * 2 + (threadIdx.x >> 7)) * ldw + blockIdx.x * 128 + (threadIdx.x & 127)] = r0;
}
__global__ void __launch_bounds__(256) k_mid(const u16* x, const float* mod, const float* part, int np, float* x1, float* y) {
  __shared__ float sx[2 * D]; __shared__ float sc[4];
  for (int i = threadIdx.x; i < 2 * D; i += 256) { int b = i >> 10, k = i & 1023; float a = 0; for (int p = 0; p < np; ++p) a += part[(p * 2 + b) * D + k];
    float v = bf2f(x[i]) + mod[b * 6 * D + 2 * D + k] * a; sx[i] = v; x1[i] = v; }
  __syncthreads();
  ln2(sx, sc);
  for (int i = threadIdx.x; i < 2 * D; i += 256) { int b = i >> 10, k = i & 1023; sx[i] = sx[i] * (1.0f + mod[b * 6 * D + 4 * D + k]) + mod[b * 6 * D + 3 * D + k]; }
  __syncthreads();
  ln2(sx, sc);
  for (int i = threadIdx.x; i < 2 * D; i += 256) y[i] = sx[i];
}
__global__ void __launch_bounds__(256) k_gelu(const float* part, int np, const u16* b1, float* hid) {
  int i = blockIdx.x * 256 + threadIdx.x; if (i >= 2 * F) return;
  int b = i / F, n = i % F; float a = bf2f(b1[n]);
  for (int p = 0; p < np; ++p) a += part[(p * 2 + b) * F + n];
  float t = tanhf(0.7978845608028654f * (a + 0.044715f * a * a * a));
  hid[i] = 0.5f * a * (1.0f + t);
}
__global__ void __launch_bounds__(256) k_fin(const float* part, int np, const u16* b2, const float* mod, const float* x1, u16* out) {
  int i = blockIdx.x * 256 + threadIdx.x; int b = i >> 10, n = i & 1023; float a = bf2f(b2[n]);
  for (int p = 0; p < np; ++p) a += part[(p * 2 + b) * D + n];
  out[i] = f2bf(x1[i] + mod[b * 6 * D + 5 * D + n] * a);
}
extern "C" void kernel_launch(void* const* d_in, const int* in_sizes, int n_in, void* d_out, int out_size, void* d_ws, size_t ws_size, hipStream_t stream) {
  const u16* x = (const u16*)d_in[0]; const u16* K = (const u16*)d_in[1]; const u16* V = (const u16*)d_in[2];
  const u16* wq = (const u16*)d_in[3]; const u16* wo = (const u16*)d_in[4];
  const u16* w1 = (const u16*)d_in[6]; const u16* b1 = (const u16*)d_in[7]; const u16* w2 = (const u16*)d_in[8]; const u16* b2 = (const u16*)d_in[9];
  const u16* c = (const u16*)d_in[10]; const u16* wmod = (const u16*)d_in[11];
  float* ws = (float*)d_ws;
  float* mod = ws; float* q = mod + 2 * 6144; float* o = q + 8192; float* x1 = o + 8192; float* y = x1 + 2048; float* hid = y + 2048;
  float* pml = hid + 8192; float* po = pml + 64 * NSPLIT * 2; float* part = po + (size_t)64 * NSPLIT * 128;
  k_mod<<<48, 256, 0, stream>>>(c, wmod, mod);
  k_q<<<32, 256, 0, stream>>>(x, mod, wq, q);
  k_attn<<<dim3(NSPLIT, HKV, 2), 256, 0, stream>>>(q, K, V, po, pml);
  k_comb<<<64, 128, 0, stream>>>(po, pml, o);
  k_gemv_part<256><<<dim3(8, 16), 256, 0, stream>>>(o, 4096, wo, D, part);
  k_mid<<<1, 256, 0, stream>>>(x, mod, part, 16, x1, y);
  k_gemv_part<128><<<dim3(28, 8), 256, 0, stream>>>(y, D, w1, F, part);
  k_gelu<<<28, 256, 0, stream>>>(part, 8, b1, hid);
  k_gemv_part<128><<<dim3(8, 28), 256, 0, stream>>>(hid, F, w2, D, part);
  k_fin<<<8, 256, 0, stream>>>(part, 28, b2, mod, x1, (u16*)d_out);
}
```

```cpp
#include <hip/hip_runtime.h>
#include <hip/hip_bf16.h>
#include <stdint.h>

#define GAS __attribute__((address_space(1)))
#define LAS __attribute__((address_space(3)))
typedef unsigned v4u __attribute__((ext_vector_type(4)));
typedef unsigned v2u __attribute__((ext_vector_type(2)));
typedef float f32x4 __attribute__((ext_vector_type(4)));
typedef __bf16 bf16x8 __attribute__((ext_vector_type(8)));
typedef __bf16 bf16x2 __attribute__((ext_vector_type(2)));
typedef short s16x4 __attribute__((ext_vector_type(4)));
typedef unsigned short u16;
typedef LAS unsigned char* lptr;

#define STAMPS 0
#define PREW 2
#define KV_AUX " nt"

namespace {

constexpr int D = 1024, SK = 32768, FF = 3584, NMOD = 6144;
constexpr float SC_LOG2 = 0.08838834764831845f * 1.4426950408889634f;
constexpr float LN_EPS = 1e-5f;

constexpr int WS_EPOCH = 0;
constexpr int WS_F1  = 4096;
constexpr int WS_F2  = WS_F1 + 4096 * 16;
constexpr int WS_F3A = WS_F2 + 8192 * 16;
constexpr int WS_F3B = WS_F3A + 8192 * 16;
constexpr int WS_ST  = WS_F3B + 1024 * 16;
constexpr int WS_H1  = WS_ST + 2048 * 16;
constexpr int WS_H2  = WS_H1 + 65536 * 16;
constexpr int NREP3  = 8;
constexpr int WS_H3  = WS_H2 + 65536 * 16;
constexpr int WS_H4  = WS_H3 + NREP3 * 512 * 16;
constexpr int WS_END = WS_H4 + 65536 * 16;
constexpr int WS_STAMP = 8 << 20;

constexpr int L_RING = 0;
constexpr int L_MISC = 131072;
constexpr int L_ACT  = L_MISC;
constexpr int L_Q    = L_ACT + 1024;
constexpr int L_RED  = L_Q + 2048;
constexpr int L_SBCB = L_RED + 4096;
constexpr int L_Y    = L_SBCB + 8192;
constexpr int L_STAT = L_Y + 4096;
constexpr int L_FAC  = L_STAT + 256;
constexpr int L_GAB  = L_FAC + 128;
constexpr int L_HID  = L_GAB + 128;
constexpr int L_FLAG = L_HID + 512;
constexpr int LDS_BYTES = 163840;
static_assert(L_FLAG + 64 <= LDS_BYTES, "LDS map");
enum { FL_SYNC = 0, FL_Q = 1, FL_STAT = 2, FL_FAC = 3, FL_ODONE = 4, FL_LASTV = 5, FL_ABORT = 6 };

constexpr unsigned SPIN_GLOBAL = 1u << 17;
constexpr unsigned SPIN_LDS = 1u << 22;

struct Params {
  const u16 *x, *K, *V, *wq, *wo, *w1, *b1, *w2, *b2, *c, *wmod;
  u16* out;
  unsigned char* ws;
};

__device__ __forceinline__ float bf_lo(unsigned u) { return __builtin_bit_cast(float, u << 16); }
__device__ __forceinline__ float bf_hi(unsigned u) { return __builtin_bit_cast(float, u & 0xffff0000u); }
__device__ __forceinline__ unsigned pack2(float lo, float hi) { bf16x2 r = {(__bf16)lo, (__bf16)hi}; return __builtin_bit_cast(unsigned, r); }
__device__ __forceinline__ float rbf(float v) { return bf_lo(pack2(v, v) & 0xffffu); }
template <int CTRL> __device__ __forceinline__ float dpp(float v) {
  return __builtin_bit_cast(float, __builtin_amdgcn_mov_dpp(__builtin_bit_cast(int, v), CTRL, 0xf, 0xf, true));
}
constexpr int XOR1 = 0xB1, XOR2 = 0x4E, ROR4 = 0x124, ROR8 = 0x128;
__device__ __forceinline__ float sum_16_32(float v) {
  auto s = __builtin_amdgcn_permlane16_swap(__float_as_uint(v), __float_as_uint(v), false, false);
  v = __uint_as_float(s[0]) + __uint_as_float(s[1]);
  auto t = __builtin_amdgcn_permlane32_swap(__float_as_uint(v), __float_as_uint(v), false, false);
  return __uint_as_float(t[0]) + __uint_as_float(t[1]);
}
__device__ __forceinline__ float max_16_32(float v) {
  auto s = __builtin_amdgcn_permlane16_swap(__float_as_uint(v), __float_as_uint(v), false, false);
  v = fmaxf(__uint_as_float(s[0]), __uint_as_float(s[1]));
  auto t = __builtin_amdgcn_permlane32_swap(__float_as_uint(v), __float_as_uint(v), false, false);
  return fmaxf(__uint_as_float(t[0]), __uint_as_float(t[1]));
}
__device__ __forceinline__ float wave_sum(float v) {
  v += dpp<XOR1>(v); v += dpp<XOR2>(v); v += dpp<ROR4>(v); v += dpp<ROR8>(v);
  return sum_16_32(v);
}
__device__ __forceinline__ f32x4 mfma32(v4u a, v4u b, f32x4 c) {
  return __builtin_amdgcn_mfma_f32_16x16x32_bf16(__builtin_bit_cast(bf16x8, a), __builtin_bit_cast(bf16x8, b), c, 0, 0, 0);
}
__device__ __forceinline__ f32x4 mfma16(v2u a, v2u b, f32x4 c) {
  return __builtin_amdgcn_mfma_f32_16x16x16bf16_1k(__builtin_bit_cast(s16x4, a), __builtin_bit_cast(s16x4, b), c, 0, 0, 0);
}
__device__ __forceinline__ v2u lds_tr(lptr p) {
  return __builtin_bit_cast(v2u, __builtin_amdgcn_ds_read_tr16_b64_v4i16((LAS s16x4*)p));
}
template <class T> __device__ __forceinline__ T lds_ld(lptr p) { return *(LAS T*)p; }
template <class T> __device__ __forceinline__ void lds_st(lptr p, T v) { *(LAS T*)p = v; }
#define WAIT_VM(n) asm volatile("s_waitcnt vmcnt(%0)" ::"i"(n) : "memory")
#define WAIT_LGKM0() asm volatile("s_waitcnt lgkmcnt(0)" ::: "memory")

#define STAMP(idx)

__device__ __forceinline__ void flag_add(lptr L, int f) {
  WAIT_LGKM0();
  if ((threadIdx.x & 63) == 0) __hip_atomic_fetch_add((LAS unsigned*)(L + L_FLAG) + f, 1u, __ATOMIC_RELAXED, __HIP_MEMORY_SCOPE_WORKGROUP);
}
__device__ __forceinline__ void flag_wait(lptr L, int f, unsigned want) {
  volatile LAS unsigned* fl = (volatile LAS unsigned*)(L + L_FLAG);
#pragma nounroll
  for (unsigned sp = 0; fl[f] < want; ++sp) {
    if (sp > SPIN_LDS || fl[FL_ABORT]) { fl[FL_ABORT] = 1; break; }
    __builtin_amdgcn_s_sleep(1);
  }
  asm volatile("" ::: "memory");
}
__device__ __forceinline__ void chain_sync(lptr L, unsigned& epoch) {
  epoch += 4;
  flag_add(L, FL_SYNC);
  flag_wait(L, FL_SYNC, epoch);
}

struct Tag { unsigned a, b; };
__device__ __forceinline__ void put(__amdgpu_buffer_rsrc_t rs, int off, unsigned lo, unsigned hi, Tag t) {
  v4u g = {lo, t.a, hi, t.b};
  __builtin_amdgcn_raw_buffer_store_b128(g, rs, off, 0, 16);
}
template <int NL>
__device__ __forceinline__ void sweep(lptr L, __amdgpu_buffer_rsrc_t rs, const int (&off)[NL], v4u (&v)[NL], Tag t) {
  volatile LAS unsigned* fl = (volatile LAS unsigned*)(L + L_FLAG);
#pragma nounroll
  for (unsigned sp = 0;; ++sp) {
#pragma unroll
    for (int k = 0; k < NL; ++k) v[k] = __builtin_amdgcn_raw_buffer_load_b128(rs, off[k], 0, 16);
    bool ok = true;
#pragma unroll
    for (int k = 0; k < NL; ++k) ok &= (v[k].y == t.a) & (v[k].w == t.b);
    if (__all(ok)) break;
    if (sp > SPIN_GLOBAL || fl[FL_ABORT]) { fl[FL_ABORT] = 1; break; }
    asm volatile("" ::: "memory");
  }
}
__device__ __forceinline__ f32x4 gval(v4u g) { return f32x4{bf_lo(g.x), bf_hi(g.x), bf_lo(g.z), bf_hi(g.z)}; }

struct Delta { unsigned m[4]; };
__device__ __forceinline__ Delta make_delta(int lane) {
  const int j = lane & 7;
  Delta d;
#pragma unroll
  for (int r = 0; r < 4; ++r) d.m[r] = (r == (j >> 1)) ? ((j & 1) ? 0xffff0000u : 0x0000ffffu) : 0u;
  return d;
}
__device__ __forceinline__ v4u delta_op(const Delta& d, unsigned pair, int hi) {
  const unsigned dup = __builtin_amdgcn_perm(pair, pair, hi ? 0x03020302u : 0x01000100u);
  return v4u{dup & d.m[0], dup & d.m[1], dup & d.m[2], dup & d.m[3]};
}

#define DMA_TILE(o0, o1, o2, o3, base, ldsaddr, AUXS)                                                    \
  do {                                                                                                   \
    unsigned keep_;                                                                                      \
    asm volatile("s_nop 4\n\ts_mov_b32 %0, m0\n\ts_mov_b32 m0, %6\n\ts_nop 0\n\t"                        \
                 "global_load_lds_dwordx4 %1, %5" AUXS "\n\t"                                            \
                 "global_load_lds_dwordx4 %2, %5 offset:1024" AUXS "\n\t"                                \
                 "global_load_lds_dwordx4 %3, %5 offset:2048" AUXS "\n\t"                                \
                 "global_load_lds_dwordx4 %4, %5 offset:3072" AUXS "\n\t"                                \
                 "s_mov_b32 m0, %0"                                                                      \
                 : "=&s"(keep_)                                                                          \
                 : "v"(o0), "v"(o1), "v"(o2), "v"(o3), "s"(base), "s"(ldsaddr)                           \
                 : "memory");                                                                            \
  } while (0)

template <bool HASW1>
__device__ __forceinline__ void stream_wave(const Params& p, lptr L, int x, int i, int w, int lane) {
  constexpr int TLAST = HASW1 ? 35 : 31;
  const int g = lane >> 4, n = lane & 15;
  const size_t key0 = (size_t)x * SK + 1024 * i + 256 * w;
  const char* Kb = (const char*)p.K + key0 * 256;
  const char* Vb = (const char*)p.V + key0 * 256;
  const int jj = 16 * x + (i - 16);
  const char* W1b = (const char*)p.w1 + (size_t)(256 * w) * (FF * 2) + jj * 64;
  const unsigned ring = (unsigned)(size_t)L + L_RING + w * 32768;
  unsigned voK[4], voV[2], voW[4];
#pragma unroll
  for (int v = 0; v < 4; ++v) voK[v] = g * 256 + ((n ^ (4 * v + g)) << 4);
#pragma unroll
  for (int v = 0; v < 2; ++v) voV[v] = g * 256 + ((n ^ (((4 * v + g) & 7) << 1)) << 4);
#pragma unroll
  for (int v = 0; v < 4; ++v) voW[v] = (16 * v + (lane >> 2)) * (FF * 2) + (((lane & 3) ^ (((lane >> 4) & 1) << 1)) << 4) - v * 1024;

#define ISSUE(t)                                                                                                     \
  do {                                                                                                               \
    const unsigned la_ = ring + ((t)&7) * 4096;                                                                      \
    if ((t) < 16) { const char* b_ = Kb + (t)*4096; DMA_TILE(voK[0], voK[1], voK[2], voK[3], b_, la_, KV_AUX); }      \
    else if ((t) < 32) { const char* b_ = Vb + ((t)-16) * 4096; DMA_TILE(voV[0], voV[1], voV[0], voV[1], b_, la_, KV_AUX); } \
    else { const char* b_ = W1b + (size_t)((t)-32) * 64 * (FF * 2); DMA_TILE(voW[0], voW[1], voW[2], voW[3], b_, la_, ""); } \
  } while (0)

#pragma unroll
  for (int t = 0; t < PREW; ++t) ISSUE(t);
#pragma unroll
  for (int t = PREW; t < 8; ++t) { WAIT_VM(4 * (PREW - 1)); ISSUE(t); }
  STAMP(1);
  flag_wait(L, FL_Q, 4);
  STAMP(2);

  v4u qf[4];
#pragma unroll
  for (int t4 = 0; t4 < 4; ++t4) qf[t4] = lds_ld<v4u>(L + L_Q + (n & 7) * 256 + t4 * 64 + g * 16);
  lptr rb = L + L_RING + w * 32768;
  int ka[4];
#pragma unroll
  for (int t4 = 0; t4 < 4; ++t4) ka[t4] = n * 256 + (((4 * t4 + g) ^ n) << 4);

  f32x4 sc[16];
#pragma unroll
  for (int t = 0; t < 16; ++t) {
    WAIT_VM(4 * 7);
    v4u kf[4];
#pragma unroll
    for (int t4 = 0; t4 < 4; ++t4) kf[t4] = lds_ld<v4u>(rb + (t & 7) * 4096 + ka[t4]);
    WAIT_LGKM0();
    ISSUE(t + 8);
    f32x4 a = {0.f, 0.f, 0.f, 0.f};
#pragma unroll
    for (int t4 = 0; t4 < 4; ++t4) a = mfma32(kf[t4], qf[t4], a);
    sc[t] = a;
  }
  STAMP(3);
  float m = -INFINITY;
#pragma unroll
  for (int t = 0; t < 16; ++t) m = fmaxf(fmaxf(fmaxf(sc[t][0], sc[t][1]), fmaxf(sc[t][2], sc[t][3])), m);
  m = max_16_32(m);
  const float mm = m * SC_LOG2;
  float lsum = 0.f;
  v2u pb[16];
#pragma unroll
  for (int t = 0; t < 16; ++t) {
    float e[4];
#pragma unroll
    for (int r = 0; r < 4; ++r) { e[r] = __builtin_amdgcn_exp2f(fmaf(sc[t][r], SC_LOG2, -mm)); lsum += e[r]; }
    pb[t] = v2u{pack2(e[0], e[1]), pack2(e[2], e[3])};
  }
  lsum = sum_16_32(lsum);
  if (lane < 8) lds_st<v2u>(L + L_STAT + (w * 8 + lane) * 8, v2u{__float_as_uint(mm), __float_as_uint(lsum)});
  flag_add(L, FL_STAT);

  int va[8];
  {
    const int q = (lane & 15) >> 2, pp = lane & 3, row = 4 * g + q;
#pragma unroll
    for (int u = 0; u < 8; ++u) va[u] = row * 256 + (((2 * u + (pp >> 1)) ^ ((row & 7) << 1)) << 4) + (pp & 1) * 8;
  }
  f32x4 o[8];
#pragma unroll
  for (int u = 0; u < 8; ++u) o[u] = f32x4{0.f, 0.f, 0.f, 0.f};
#pragma unroll
  for (int t = 16; t < 32; ++t) {
    WAIT_VM(4 * ((t + 7 < TLAST ? t + 7 : TLAST) - t));
    v2u vf[8];
#pragma unroll
    for (int u = 0; u < 8; ++u) vf[u] = lds_tr(rb + (t & 7) * 4096 + va[u]);
    WAIT_LGKM0();
    if (t + 8 <= TLAST) ISSUE(t + 8);
    if (t == 23) flag_add(L, FL_LASTV);
#pragma unroll
    for (int u = 0; u < 8; ++u) o[u] = mfma16(vf[u], pb[t - 16], o[u]);
  }
  STAMP(4);
  flag_wait(L, FL_FAC, 1);
  const float f = lds_ld<float>(L + L_FAC + (w * 8 + (n & 7)) * 4);
  if (n < 8) {
#pragma unroll
    for (int u = 0; u < 8; ++u) lds_st<f32x4>(rb + 4 * 4096 + n * 528 + (16 * u + 4 * g) * 4, o[u] * f);
  }
  flag_add(L, FL_ODONE);
  STAMP(5);
  WAIT_VM(0);
  STAMP(6);
#undef ISSUE
}

__device__ __forceinline__ f32x4 red4(lptr L, int lane) {
  f32x4 s = lds_ld<f32x4>(L + L_RED + lane * 16);
#pragma unroll
  for (int ww = 1; ww < 4; ++ww) s += lds_ld<f32x4>(L + L_RED + ww * 1024 + lane * 16);
  return s;
}

__device__ __forceinline__ f32x4 gemv_tile128(lptr L, const v4u (&W)[16], const Delta& dl, int cw, int lane) {
  const int g = lane >> 4, b = (lane >> 3) & 1;
  const v4u a0 = lds_ld<v4u>(L + L_ACT + ((b * 4 + cw) * 4 + g) * 32), a1 = lds_ld<v4u>(L + L_ACT + ((b * 4 + cw) * 4 + g) * 32 + 16);
  f32x4 acc = {0.f, 0.f, 0.f, 0.f};
#pragma unroll
  for (int u = 0; u < 8; ++u) acc = mfma32(delta_op(dl, a0[u >> 1], u & 1), W[u], acc);
#pragma unroll
  for (int u = 0; u < 8; ++u) acc = mfma32(delta_op(dl, a1[u >> 1], u & 1), W[8 + u], acc);
  return acc;
}
__device__ __forceinline__ void gemv_rows32(const v4u (&W)[16], const Delta& dl, v4u av, f32x4& acc0, f32x4& acc1) {
  acc0 = f32x4{0.f, 0.f, 0.f, 0.f}; acc1 = acc0;
#pragma unroll
  for (int u = 0; u < 8; ++u) {
    const v4u a = delta_op(dl, av[u >> 1], u & 1);
    acc0 = mfma32(a, W[u], acc0);
    acc1 = mfma32(a, W[8 + u], acc1);
  }
}
__device__ __forceinline__ void load_rows32(v4u (&W)[16], const u16* Wg, int row0, int cw, int lane) {
  const int g = lane >> 4, c = lane & 15;
#pragma unroll
  for (int cb = 0; cb < 2; ++cb)
#pragma unroll
    for (int u = 0; u < 8; ++u) W[cb * 8 + u] = *(const GAS v4u*)(Wg + (size_t)(row0 + 4 * u + g) * D + (2 * cw + cb) * 128 + 8 * c);
  __builtin_amdgcn_sched_barrier(0);
}
__device__ __forceinline__ void load_tile128(v4u (&W)[16], const u16* Wg, int ld, int row0, int col0, int cw, int lane) {
  const int g = lane >> 4, c = lane & 15;
#pragma unroll
  for (int u = 0; u < 16; ++u) W[u] = *(const GAS v4u*)(Wg + (size_t)(row0 + 64 * cw + 4 * u + g) * ld + col0 + 8 * c);
  __builtin_amdgcn_sched_barrier(0);
}
__device__ __forceinline__ void put_owner(__amdgpu_buffer_rsrc_t rs, int base, int prod, int cw, int lane, f32x4 acc0, f32x4 acc1, Tag tg) {
  const int g = lane >> 4, c = lane & 15;
  put(rs, base + (((32 * cw + c) * 128 + prod) * 4 + g) * 16, pack2(acc0[0], acc0[1]), pack2(acc0[2], acc0[3]), tg);
  put(rs, base + (((32 * cw + 16 + c) * 128 + prod) * 4 + g) * 16, pack2(acc1[0], acc1[1]), pack2(acc1[2], acc1[3]), tg);
}
template <int NP>
__device__ __forceinline__ f32x4 gather_owner(lptr L, __amdgpu_buffer_rsrc_t rs, int base, int owner, int cw, int lane, Tag tg) {
  const int t = cw * 64 + lane;
  int off[2]; v4u v[2];
  const int p1 = (t >> 2) + 64;
  off[0] = base + ((owner * 128 + (t >> 2)) * 4 + (t & 3)) * 16;
  off[1] = p1 < NP ? base + ((owner * 128 + p1) * 4 + (t & 3)) * 16 : off[0];
  sweep<2>(L, rs, off, v, tg);
  f32x4 s = gval(v[0]);
  if (p1 < NP) s += gval(v[1]);
#pragma unroll
  for (int e = 0; e < 4; ++e) { float a = s[e]; a += dpp<ROR4>(a); a += dpp<ROR8>(a); s[e] = sum_16_32(a); }
  if (lane < 4) lds_st<f32x4>(L + L_RED + (cw * 4 + lane) * 16, s);
  WAIT_LGKM0();
  __builtin_amdgcn_s_barrier();
  f32x4 r = lds_ld<f32x4>(L + L_RED + (lane & 3) * 16);
#pragma unroll
  for (int ww = 1; ww < 4; ++ww) r += lds_ld<f32x4>(L + L_RED + (ww * 4 + (lane & 3)) * 16);
  return r;
}
__device__ __forceinline__ float silu(float v) { return v / (1.0f + __expf(-v)); }
__device__ __forceinline__ float gelu_tanh(float a) {
  const float u = 0.7978845608028654f * (a + 0.044715f * a * a * a);
  const float e = __expf(2.0f * u);
  const float th = 1.0f - 2.0f / (e + 1.0f);
  return 0.5f * a * (1.0f + th);
}

__device__ __forceinline__ void gather_q(lptr L, __amdgpu_buffer_rsrc_t rs, int x, int ct, Tag tg) {
  int off[4]; v4u v[4];
#pragma unroll
  for (int k = 0; k < 4; ++k) off[k] = WS_F2 + ((k * 2 + (x >> 2)) * 1024 + (x & 3) * 256 + ct) * 16;
  sweep<4>(L, rs, off, v, tg);
  const f32x4 s = (gval(v[0]) + gval(v[1])) + (gval(v[2]) + gval(v[3]));
  lds_st<v2u>(L + L_Q + ct * 8, v2u{pack2(s[0], s[1]), pack2(s[2], s[3])});
  flag_add(L, FL_Q);
}
__device__ __forceinline__ void stats_exchange(lptr L, __amdgpu_buffer_rsrc_t rs, int x, int i, int lane, Tag tg) {
  flag_wait(L, FL_STAT, 4);
  const int h = lane & 7;
  float mw[4], lw[4];
#pragma unroll
  for (int w = 0; w < 4; ++w) { const v2u s = lds_ld<v2u>(L + L_STAT + (w * 8 + h) * 8); mw[w] = __uint_as_float(s.x); lw[w] = __uint_as_float(s.y); }
  const float mc = fmaxf(fmaxf(mw[0], mw[1]), fmaxf(mw[2], mw[3]));
  float lc = 0.f;
#pragma unroll
  for (int w = 0; w < 4; ++w) lc += lw[w] * __builtin_amdgcn_exp2f(mw[w] - mc);
  if (lane < 8) put(rs, WS_ST + ((x * 32 + i) * 8 + h) * 16, __float_as_uint(mc), __float_as_uint(lc), tg);
  int off[4]; v4u v[4];
#pragma unroll
  for (int k = 0; k < 4; ++k) off[k] = WS_ST + ((x * 32 + (lane >> 3) + 8 * k) * 8 + h) * 16;
  sweep<4>(L, rs, off, v, tg);
  float M = -INFINITY;
#pragma unroll
  for (int k = 0; k < 4; ++k) M = fmaxf(M, __uint_as_float(v[k].x));
  M = fmaxf(M, dpp<ROR8>(M));
  M = max_16_32(M);
  float S = 0.f;
#pragma unroll
  for (int k = 0; k < 4; ++k) S += __uint_as_float(v[k].z) * __builtin_amdgcn_exp2f(__uint_as_float(v[k].x) - M);
  S += dpp<ROR8>(S);
  S = sum_16_32(S);
  const float inv = 1.0f / S;
  if (lane < 32) {
    const int w = lane >> 3;
    const float mws = w == 0 ? mw[0] : w == 1 ? mw[1] : w == 2 ? mw[2] : mw[3];
    lds_st<float>(L + L_FAC + lane * 4, __builtin_amdgcn_exp2f(mws - M) * inv);
  }
  flag_add(L, FL_FAC);
}
__device__ __forceinline__ void publish_partial(lptr L, __amdgpu_buffer_rsrc_t rs, int x, int i, int ct, Tag tg) {
  flag_wait(L, FL_ODONE, 4);
  const int h = ct >> 5, gi = ct & 31;
  f32x4 s = lds_ld<f32x4>(L + L_RING + 4 * 4096 + h * 528 + gi * 16);
#pragma unroll
  for (int w = 1; w < 4; ++w) s += lds_ld<f32x4>(L + L_RING + w * 32768 + 4 * 4096 + h * 528 + gi * 16);
  put(rs, WS_H1 + (((((x >> 2) * 32 + (x & 3) * 8 + h) * 4 + (gi >> 3)) * 32 + i) * 8 + (gi & 7)) * 16, pack2(s[0], s[1]), pack2(s[2], s[3]), tg);
}

__device__ __forceinline__ void chain_a(const Params& p, lptr L, __amdgpu_buffer_rsrc_t rs, int x, int i, int cw, int lane, Tag tg) {
  const int a = 16 * x + i, ct = cw * 64 + lane, g = lane >> 4;
  const Delta dl = make_delta(lane);
  unsigned sync = 0;
  const int cg = a >> 2, ks = a & 3, row0 = 256 * ks + 64 * cw;
  v4u W[16];
  unsigned cpair = *(const GAS unsigned*)(p.c + (lane >> 5) * D + row0 + 2 * (lane & 31));
  v2u xown = *(const GAS v2u*)(p.x + ((lane >> 1) & 1) * D + 8 * a + 4 * (lane & 1));
  v2u b2own = *(const GAS v2u*)(p.b2 + 8 * a + 4 * (lane & 1));
  __builtin_amdgcn_sched_barrier(0);
#pragma unroll
  for (int u = 0; u < 8; ++u) W[u] = *(const GAS v4u*)(p.wmod + (size_t)(row0 + 8 * u + (lane >> 3)) * NMOD + 64 * cg + 8 * (lane & 7));
  __builtin_amdgcn_sched_barrier(0);
  asm volatile("" : "+v"(cpair), "+v"(xown), "+v"(b2own), "+v"(tg.a));
  tg.a += 1u; tg.b = ~tg.a;
  {
    const int rr = 2 * (lane & 31), b = lane >> 5;
    lds_st<unsigned>(L + L_ACT + cw * 256 + (b * 64 + ((rr >> 1) & 3) * 16 + (rr >> 3) * 2) * 2, pack2(silu(bf_lo(cpair)), silu(bf_hi(cpair))));
    const int bb = (lane >> 3) & 1;
    const v4u a0 = lds_ld<v4u>(L + L_ACT + cw * 256 + (bb * 64 + g * 16) * 2), a1 = lds_ld<v4u>(L + L_ACT + cw * 256 + (bb * 64 + g * 16) * 2 + 16);
    f32x4 acc0 = {0.f, 0.f, 0.f, 0.f}, acc1 = acc0;
#pragma unroll
    for (int u = 0; u < 8; ++u) {
      const unsigned pr = u < 4 ? a0[u] : a1[u - 4];
      acc0 = mfma32(delta_op(dl, pr, 0), W[u], acc0);
      acc1 = mfma32(delta_op(dl, pr, 1), W[u], acc1);
    }
    f32x4 t = (lane & 8) ? acc1 : acc0;
#pragma unroll
    for (int e = 0; e < 4; ++e) t[e] += dpp<ROR8>(t[e]);
    lds_st<f32x4>(L + L_RED + cw * 1024 + lane * 16, t);
    chain_sync(L, sync);
    if (cw == 0 && (lane & 8) == 0) {
      const f32x4 s = red4(L, lane);
      put(rs, WS_F1 + ((ks * 2 + (g >> 1)) * 512 + 16 * cg + 2 * (lane & 7) + (g & 1)) * 16, pack2(s[0], s[1]), pack2(s[2], s[3]), tg);
    }
  }
  STAMP(1);
  load_rows32(W, p.wo, 32 * a, cw, lane);
  gather_q(L, rs, x, ct, tg);
  STAMP(2);
  if (cw == 0) {
    const int v = (lane >> 2) & 3, b = (lane >> 1) & 1, half = lane & 1;
    int off[1] = {WS_F3A + (((lane >> 4) * 2 + b) * 1024 + v * 256 + 2 * a + half) * 16};
    v4u gv[1];
    sweep<1>(L, rs, off, gv, tg);
    f32x4 s = gval(gv[0]);
#pragma unroll
    for (int e = 0; e < 4; ++e) s[e] = sum_16_32(s[e]);
    const unsigned lo = pack2(s[0], s[1]), hi = pack2(s[2], s[3]);
    if (lane < 16) {
      if (v == 1 || v == 2) put(rs, WS_F3B + ((b * 2 + v - 1) * 256 + 2 * a + half) * 16, lo, hi, tg);
      else lds_st<f32x4>(L + L_GAB + ((v ? 1 : 0) * 4 + b * 2 + half) * 16, f32x4{bf_lo(lo), bf_hi(lo), bf_lo(hi), bf_hi(hi)});
    }
    STAMP(3);
    stats_exchange(L, rs, x, i, lane, tg);
  }
  STAMP(4);
  publish_partial(L, rs, x, i, ct, tg);
  STAMP(5);
  {
    const int b = ct >> 7, sl = (ct & 127) >> 3, gi = ct & 7;
    int off[2]; v4u v[2];
#pragma unroll
    for (int k = 0; k < 2; ++k) off[k] = WS_H1 + ((((b * 32 + (a >> 2)) * 4 + (a & 3)) * 32 + sl + 16 * k) * 8 + gi) * 16;
    sweep<2>(L, rs, off, v, tg);
    STAMP(6);
    f32x4 s = gval(v[0]) + gval(v[1]);
#pragma unroll
    for (int e = 0; e < 4; ++e) { float t = s[e]; t += dpp<ROR8>(t); s[e] = sum_16_32(t); }
    if (lane < 8) {
#pragma unroll
      for (int e = 0; e < 4; ++e) lds_st<float>(L + L_RED + ((cw * 4 + e) * 8 + lane) * 4, s[e]);
    }
    WAIT_LGKM0();
    __builtin_amdgcn_s_barrier();
    const int bb = (lane >> 3) & 1;
    lptr r0 = L + L_RED + ((2 * bb * 4 + g) * 8) * 4, r1 = r0 + 128;
    const f32x4 o0 = lds_ld<f32x4>(r0) + lds_ld<f32x4>(r1), o1 = lds_ld<f32x4>(r0 + 16) + lds_ld<f32x4>(r1 + 16);
    const v4u av = {pack2(o0[0], o0[1]), pack2(o0[2], o0[3]), pack2(o1[0], o1[1]), pack2(o1[2], o1[3])};
    f32x4 acc0, acc1;
    gemv_rows32(W, dl, av, acc0, acc1);
    put_owner(rs, WS_H2, a, cw, lane, acc0, acc1, tg);
    WAIT_LGKM0();
    __builtin_amdgcn_s_barrier();
  }
  STAMP(7);
  const f32x4 attn = gather_owner<128>(L, rs, WS_H2, a, cw, lane, tg);
  STAMP(8);
  const f32x4 ga = lds_ld<f32x4>(L + L_GAB + (lane & 3) * 16), gb = lds_ld<f32x4>(L + L_GAB + (4 + (lane & 3)) * 16);
  const f32x4 xv = {bf_lo(xown.x), bf_hi(xown.x), bf_lo(xown.y), bf_hi(xown.y)};
  f32x4 x1;
#pragma unroll
  for (int e = 0; e < 4; ++e) x1[e] = xv[e] + ga[e] * rbf(attn[e]);
  const unsigned x1lo = pack2(x1[0], x1[1]), x1hi = pack2(x1[2], x1[3]);
  if (cw == 0 && lane < 4 * NREP3) put(rs, WS_H3 + (((lane >> 2) * 128 + a) * 4 + (lane & 3)) * 16, x1lo, x1hi, tg);
  STAMP(9);
  WAIT_LGKM0();
  __builtin_amdgcn_s_barrier();
  const f32x4 mlp = gather_owner<112>(L, rs, WS_H4, a, cw, lane, tg);
  STAMP(10);
  if (cw == 0 && lane < 4) {
    const f32x4 x1r = {bf_lo(x1lo), bf_hi(x1lo), bf_lo(x1hi), bf_hi(x1hi)};
    const f32x4 bv = {bf_lo(b2own.x), bf_hi(b2own.x), bf_lo(b2own.y), bf_hi(b2own.y)};
    f32x4 r;
#pragma unroll
    for (int e = 0; e < 4; ++e) r[e] = x1r[e] + gb[e] * (mlp[e] + bv[e]);
    if (*(volatile LAS unsigned*)(L + L_FLAG + FL_ABORT * 4)) r = f32x4{NAN, NAN, NAN, NAN};
    *(GAS v2u*)(p.out + (lane >> 1) * D + 8 * a + 4 * (lane & 1)) = v2u{pack2(r[0], r[1]), pack2(r[2], r[3])};
    if (a == 0 && lane == 0) __hip_atomic_store((GAS unsigned*)(p.ws + WS_EPOCH), tg.a, __ATOMIC_RELAXED, __HIP_MEMORY_SCOPE_AGENT);
  }
  STAMP(11);
}

__device__ __forceinline__ void ln_rows(f32x4 (&v)[4]) {
  float s = 0.f;
#pragma unroll
  for (int k = 0; k < 4; ++k) s += (v[k][0] + v[k][1]) + (v[k][2] + v[k][3]);
  const float mean = wave_sum(s) * (1.0f / D);
  float q = 0.f;
#pragma unroll
  for (int k = 0; k < 4; ++k) { v[k] -= mean; q += (v[k][0] * v[k][0] + v[k][1] * v[k][1]) + (v[k][2] * v[k][2] + v[k][3] * v[k][3]); }
  const float r = 1.0f / sqrtf(wave_sum(q) * (1.0f / D) + LN_EPS);
#pragma unroll
  for (int k = 0; k < 4; ++k) v[k] *= r;
}
__device__ __forceinline__ void chain_b(const Params& p, lptr L, __amdgpu_buffer_rsrc_t rs, int x, int i, int cw, int lane, Tag tg) {
  const int j = 16 * x + (i - 16), ct = cw * 64 + lane, g = lane >> 4;
  const Delta dl = make_delta(lane);
  unsigned sync = 0;
  const int ks = x & 3, cgrp = (x >> 2) * 16 + (i - 16);
  v4u W[16];
  unsigned cpair = *(const GAS unsigned*)(p.c + (lane >> 5) * D + 256 * ks + 64 * cw + 2 * (lane & 31));
  unsigned b1u = *(const GAS u16*)(p.b1 + 32 * (j < 112 ? j : 0) + (lane & 31));
  v4u xa = *(const GAS v4u*)(p.x + (cw & 1) * D + 16 * lane), xb = *(const GAS v4u*)(p.x + (cw & 1) * D + 16 * lane + 8);
  v2u xs = *(const GAS v2u*)(p.x + (cw & 1) * D + 256 * ks + 4 * lane);
  __builtin_amdgcn_sched_barrier(0);
  load_tile128(W, p.wq, 4096, 256 * ks, 128 * cgrp, cw, lane);
  asm volatile("" : "+v"(cpair), "+v"(b1u), "+v"(xa), "+v"(xb), "+v"(xs), "+v"(tg.a));
  tg.a += 1u; tg.b = ~tg.a;
  if (cw < 2) {
    const int b = cw;
    float xv[16];
#pragma unroll
    for (int e = 0; e < 4; ++e) { xv[2 * e] = bf_lo(xa[e]); xv[2 * e + 1] = bf_hi(xa[e]); xv[8 + 2 * e] = bf_lo(xb[e]); xv[9 + 2 * e] = bf_hi(xb[e]); }
    float s = 0.f;
#pragma unroll
    for (int e = 0; e < 16; ++e) s += xv[e];
    const float mean = wave_sum(s) * (1.0f / D);
    float q = 0.f;
#pragma unroll
    for (int e = 0; e < 16; ++e) { const float d = xv[e] - mean; q += d * d; }
    const float rstd = 1.0f / sqrtf(wave_sum(q) * (1.0f / D) + LN_EPS);
    int off[8]; v4u v[8];
#pragma unroll
    for (int k = 0; k < 8; ++k) off[k] = WS_F1 + (((k >> 1) * 2 + b) * 512 + (k & 1) * 256 + 64 * ks + lane) * 16;
    sweep<8>(L, rs, off, v, tg);
    const f32x4 sa = (gval(v[0]) + gval(v[2])) + (gval(v[4]) + gval(v[6])), ca = (gval(v[1]) + gval(v[3])) + (gval(v[5]) + gval(v[7]));
    const float xs4[4] = {bf_lo(xs.x), bf_hi(xs.x), bf_lo(xs.y), bf_hi(xs.y)};
#pragma unroll
    for (int e = 0; e < 4; ++e) {
      const float h = (xs4[e] - mean) * rstd * (1.0f + rbf(ca[e])) + rbf(sa[e]);
      lds_st<u16>(L + L_ACT + (((b * 4 + (lane >> 4)) * 4 + e) * 16 + (lane & 15)) * 2, (u16)pack2(h, h));
    }
  }
  chain_sync(L, sync);
  STAMP(1);
  {
    const f32x4 acc = gemv_tile128(L, W, dl, cw, lane);
    lds_st<f32x4>(L + L_RED + cw * 1024 + lane * 16, acc);
    chain_sync(L, sync);
    if (cw == 0) {
      const f32x4 s = red4(L, lane);
      put(rs, WS_F2 + ((ks * 2 + (g >> 1)) * 1024 + 32 * cgrp + 2 * (lane & 15) + (g & 1)) * 16, pack2(s[0], s[1]), pack2(s[2], s[3]), tg);
    }
  }
  STAMP(2);
  gather_q(L, rs, x, ct, tg);
  STAMP(3);
  load_tile128(W, p.wmod, NMOD, 256 * ks, 2048 + 128 * cgrp, cw, lane);
  {
    const int rr = 2 * (lane & 31), b = lane >> 5;
    lptr ap = L + L_ACT + (((b * 4 + cw) * 4 + (rr & 3)) * 16 + (rr >> 2)) * 2;
    const unsigned sp = pack2(silu(bf_lo(cpair)), silu(bf_hi(cpair)));
    chain_sync(L, sync);
    lds_st<u16>(ap, (u16)sp);
    lds_st<u16>(ap + 32, (u16)(sp >> 16));
    const f32x4 acc = gemv_tile128(L, W, dl, cw, lane);
    lds_st<f32x4>(L + L_RED + cw * 1024 + lane * 16, acc);
    chain_sync(L, sync);
    if (cw == 0) {
      const f32x4 s = red4(L, lane);
      put(rs, WS_F3A + ((ks * 2 + (g >> 1)) * 1024 + 32 * cgrp + 2 * (lane & 15) + (g & 1)) * 16, pack2(s[0], s[1]), pack2(s[2], s[3]), tg);
      STAMP(4);
      stats_exchange(L, rs, x, i, lane, tg);
    }
  }
  STAMP(5);
  if (j < 112) {
    int off[4]; v4u v[4];
#pragma unroll
    for (int k = 0; k < 4; ++k) off[k] = WS_F3B + (k * 256 + ct) * 16;
    sweep<4>(L, rs, off, v, tg);
#pragma unroll
    for (int k = 0; k < 4; ++k) lds_st<v2u>(L + L_SBCB + (k * 1024 + 4 * ct) * 2, v2u{v[k].x, v[k].z});
    flag_wait(L, FL_LASTV, 4);
    load_rows32(W, p.w2, 32 * j, cw, lane);
  }
  STAMP(6);
  publish_partial(L, rs, x, i, ct, tg);
  STAMP(7);
  if (j >= 112) return;
  if (cw < 2) {
    const int b = cw;
    int off[4]; v4u v[4];
#pragma unroll
    for (int k = 0; k < 4; ++k) off[k] = WS_H3 + ((x * 128 + (k >> 1) * 64 + lane) * 4 + 2 * b + (k & 1)) * 16;
    sweep<4>(L, rs, off, v, tg);
    STAMP(8);
    f32x4 y[4];
#pragma unroll
    for (int k = 0; k < 4; ++k) y[k] = gval(v[k]);
    ln_rows(y);
#pragma unroll
    for (int k = 0; k < 4; ++k) {
      const int col = 8 * ((k >> 1) * 64 + lane) + 4 * (k & 1);
      const v2u sb = lds_ld<v2u>(L + L_SBCB + ((b * 2 + 0) * 1024 + col) * 2), cb = lds_ld<v2u>(L + L_SBCB + ((b * 2 + 1) * 1024 + col) * 2);
      y[k][0] = y[k][0] * (1.0f + bf_lo(cb.x)) + bf_lo(sb.x);
      y[k][1] = y[k][1] * (1.0f + bf_hi(cb.x)) + bf_hi(sb.x);
      y[k][2] = y[k][2] * (1.0f + bf_lo(cb.y)) + bf_lo(sb.y);
      y[k][3] = y[k][3] * (1.0f + bf_hi(cb.y)) + bf_hi(sb.y);
    }
    ln_rows(y);
#pragma unroll
    for (int k = 0; k < 4; ++k) {
      const int gq = 2 * ((k >> 1) * 64 + lane) + (k & 1);
      const int pos = (gq >> 3) * 32 + (gq & 3) * 8 + ((gq >> 2) & 1) * 4;
      lds_st<v2u>(L + L_Y + (b * D + pos) * 2, v2u{pack2(y[k][0], y[k][1]), pack2(y[k][2], y[k][3])});
    }
  }
  WAIT_LGKM0();
  __builtin_amdgcn_s_barrier();
  STAMP(9);
  {
    f32x4 acc[2] = {{0.f, 0.f, 0.f, 0.f}, {0.f, 0.f, 0.f, 0.f}};
    const int q = (lane & 15) >> 2, pp = lane & 3;
#pragma unroll
    for (int kk = 0; kk < 8; ++kk) {
      const v4u ya = lds_ld<v4u>(L + L_Y + ((lane & 1) * D + 256 * cw + 32 * kk + 8 * g) * 2);
#pragma unroll
      for (int ch = 0; ch < 2; ++ch) {
        v2u w[2];
#pragma unroll
        for (int hh = 0; hh < 2; ++hh) {
          const int row = (kk & 1) * 32 + 16 * hh + 4 * g + q;
          const int pc = (2 * ch + (pp >> 1)) ^ (((row >> 2) & 1) << 1);
          w[hh] = lds_tr(L + L_RING + cw * 32768 + (kk >> 1) * 4096 + row * 64 + pc * 16 + (pp & 1) * 8);
        }
        acc[ch] = mfma32(ya, v4u{w[0].x, w[0].y, w[1].x, w[1].y}, acc[ch]);
      }
    }
    if (lane < 16) {
#pragma unroll
      for (int ch = 0; ch < 2; ++ch)
#pragma unroll
        for (int b = 0; b < 2; ++b) lds_st<float>(L + L_RED + (cw * 64 + b * 32 + ch * 16 + lane) * 4, acc[ch][b]);
    }
    WAIT_LGKM0();
    __builtin_amdgcn_s_barrier();
    float a = bf_lo(b1u);
#pragma unroll
    for (int ww = 0; ww < 4; ++ww) a += lds_ld<float>(L + L_RED + (ww * 64 + lane) * 4);
    const float hv = gelu_tanh(rbf(a));
    const int k = lane & 31;
    lds_st<u16>(L + L_HID + cw * 128 + ((lane >> 5) * 32 + (k & 3) * 8 + (k >> 2)) * 2, (u16)pack2(hv, hv));
    const v4u av = lds_ld<v4u>(L + L_HID + cw * 128 + (((lane >> 3) & 1) * 32 + g * 8) * 2);
    f32x4 acc0, acc1;
    gemv_rows32(W, dl, av, acc0, acc1);
    put_owner(rs, WS_H4, j, cw, lane, acc0, acc1, tg);
  }
  STAMP(10);
}

__global__ void __launch_bounds__(512, 2) fused_decode_block(Params p) {
  extern __shared__ __attribute__((aligned(16))) unsigned char smem[];
  lptr L = (lptr)smem;
  const int tid = threadIdx.x, lane = tid & 63, wave = __builtin_amdgcn_readfirstlane(tid >> 6);
  const int x = blockIdx.x & 7, i = blockIdx.x >> 3;
  STAMP(0);
  if (tid < 16) *(LAS unsigned*)(L + L_FLAG + tid * 4) = 0u;
  WAIT_LGKM0();
  __builtin_amdgcn_s_barrier();
  if (wave < 4) {
    if (i >= 16 && 16 * x + (i - 16) < 112) stream_wave<true>(p, L, x, i, wave, lane);
    else stream_wave<false>(p, L, x, i, wave, lane);
    return;
  }
  __builtin_amdgcn_s_setprio(2);
  const __amdgpu_buffer_rsrc_t rs = __builtin_amdgcn_make_buffer_rsrc((void*)p.ws, 0, WS_END, 0x00020000);
  const Tag tg = {__hip_atomic_load((GAS unsigned*)(p.ws + WS_EPOCH), __ATOMIC_RELAXED, __HIP_MEMORY_SCOPE_AGENT), 0u};
  if (i < 16) chain_a(p, L, rs, x, i, wave - 4, lane, tg);
  else chain_b(p, L, rs, x, i, wave - 4, lane, tg);
}

}

extern "C" void kernel_launch(void* const* d_in, const int* in_sizes, int n_in, void* d_out, int out_size, void* d_ws, size_t ws_size,
                              hipStream_t stream) {
  Params p;
  p.x = (const u16*)d_in[0];
  p.K = (const u16*)d_in[1];
  p.V = (const u16*)d_in[2];
  p.wq = (const u16*)d_in[3];
  p.wo = (const u16*)d_in[4];
  p.w1 = (const u16*)d_in[6];
  p.b1 = (const u16*)d_in[7];
  p.w2 = (const u16*)d_in[8];
  p.b2 = (const u16*)d_in[9];
  p.c = (const u16*)d_in[10];
  p.wmod = (const u16*)d_in[11];
  p.out = (u16*)d_out;
  p.ws = (unsigned char*)d_ws;
  (void)hipFuncSetAttribute((const void*)fused_decode_block, hipFuncAttributeMaxDynamicSharedMemorySize, LDS_BYTES);
  hipLaunchKernelGGL(fused_decode_block, dim3(256), dim3(512), LDS_BYTES, stream, p);
}
```

```cpp
#define GATE 0
#define W_NT 0
#define W1_AUX ""
#include <hip/hip_runtime.h>
#include <hip/hip_bf16.h>
#include <stdint.h>

#define GAS __attribute__((address_space(1)))
#define LAS __attribute__((address_space(3)))
typedef unsigned v4u __attribute__((ext_vector_type(4)));
typedef unsigned v2u __attribute__((ext_vector_type(2)));
typedef float f32x4 __attribute__((ext_vector_type(4)));
typedef __bf16 bf16x8 __attribute__((ext_vector_type(8)));
typedef __bf16 bf16x2 __attribute__((ext_vector_type(2)));
typedef short s16x4 __attribute__((ext_vector_type(4)));
typedef unsigned short u16;
typedef LAS unsigned char* lptr;

#define STAMPS 0
#define PREW 2
#define LOOKA 8
#define SHRINK 16
#define CAN_FRONT 2
#define CAN_TAIL 1
#define CAN_SLOW 16
#define GOA 3
#define GOB 7
#define PREWB PREW
#define GATEB_Q 0
#define GATEA_Q 0
#define WO_LATE 0
#define KV_AUX " nt"

namespace {

constexpr int D = 1024, SK = 32768, FF = 3584, NMOD = 6144;
constexpr float SC_LOG2 = 0.08838834764831845f * 1.4426950408889634f;
constexpr float LN_EPS = 1e-5f;

constexpr int WS_EPOCH = 0;
constexpr int WS_F1  = 4096;
constexpr int WS_F2  = WS_F1 + 4096 * 16;
constexpr int WS_F3A = WS_F2 + 8192 * 16;
constexpr int WS_F3B = WS_F3A + 8192 * 16;
constexpr int WS_ST  = WS_F3B + 1024 * 16;
constexpr int WS_H1  = WS_ST + 2048 * 16;
constexpr int WS_H2  = WS_H1 + 65536 * 16;
constexpr int NREP3  = 8;
constexpr int WS_H3  = WS_H2 + 65536 * 16;
constexpr int WS_H4  = WS_H3 + NREP3 * 512 * 16;
constexpr int WS_END = WS_H4 + 65536 * 16;
constexpr int WS_STAMP = 8 << 20;

constexpr int L_RING = 0;
constexpr int L_MISC = 131072;
constexpr int L_ACT  = L_MISC;
constexpr int L_Q    = L_ACT + 1024;
constexpr int L_RED  = L_Q + 2048;
constexpr int L_SBCB = L_RED + 4096;
constexpr int L_Y    = L_SBCB + 8192;
constexpr int L_STAT = L_Y + 4096;
constexpr int L_FAC  = L_STAT + 256;
constexpr int L_GAB  = L_FAC + 128;
constexpr int L_HID  = L_GAB + 128;
constexpr int L_FLAG = L_HID + 512;
constexpr int LDS_BYTES = 163840;
static_assert(L_FLAG + 64 <= LDS_BYTES, "LDS map");
enum { FL_SYNC = 0, FL_Q = 1, FL_STAT = 2, FL_FAC = 3, FL_ODONE = 4, FL_LASTV = 5, FL_ABORT = 6, FL_GO = 7 };

constexpr unsigned SPIN_GLOBAL = 1u << 17;
constexpr unsigned SPIN_LDS = 1u << 22;

struct Params {
  const u16 *x, *K, *V, *wq, *wo, *w1, *b1, *w2, *b2, *c, *wmod;
  u16* out;
  unsigned char* ws;
};

__device__ __forceinline__ float bf_lo(unsigned u) { return __builtin_bit_cast(float, u << 16); }
__device__ __forceinline__ float bf_hi(unsigned u) { return __builtin_bit_cast(float, u & 0xffff0000u); }
__device__ __forceinline__ unsigned pack2(float lo, float hi) { bf16x2 r = {(__bf16)lo, (__bf16)hi}; return __builtin_bit_cast(unsigned, r); }
__device__ __forceinline__ float rbf(float v) { return bf_lo(pack2(v, v) & 0xffffu); }
template <int CTRL> __device__ __forceinline__ float dpp(float v) {
  return __builtin_bit_cast(float, __builtin_amdgcn_mov_dpp(__builtin_bit_cast(int, v), CTRL, 0xf, 0xf, true));
}
constexpr int XOR1 = 0xB1, XOR2 = 0x4E, ROR4 = 0x124, ROR8 = 0x128;
__device__ __forceinline__ float sum_16_32(float v) {
  auto s = __builtin_amdgcn_permlane16_swap(__float_as_uint(v), __float_as_uint(v), false, false);
  v = __uint_as_float(s[0]) + __uint_as_float(s[1]);
  auto t = __builtin_amdgcn_permlane32_swap(__float_as_uint(v), __float_as_uint(v), false, false);
  return __uint_as_float(t[0]) + __uint_as_float(t[1]);
}
__device__ __forceinline__ float max_16_32(float v) {
  auto s = __builtin_amdgcn_permlane16_swap(__float_as_uint(v), __float_as_uint(v), false, false);
  v = fmaxf(__uint_as_float(s[0]), __uint_as_float(s[1]));
  auto t = __builtin_amdgcn_permlane32_swap(__float_as_uint(v), __float_as_uint(v), false, false);
  return fmaxf(__uint_as_float(t[0]), __uint_as_float(t[1]));
}
__device__ __forceinline__ float wave_sum(float v) {
  v += dpp<XOR1>(v); v += dpp<XOR2>(v); v += dpp<ROR4>(v); v += dpp<ROR8>(v);
  return sum_16_32(v);
}
__device__ __forceinline__ f32x4 mfma32(v4u a, v4u b, f32x4 c) {
  return __builtin_amdgcn_mfma_f32_16x16x32_bf16(__builtin_bit_cast(bf16x8, a), __builtin_bit_cast(bf16x8, b), c, 0, 0, 0);
}
__device__ __forceinline__ f32x4 mfma16(v2u a, v2u b, f32x4 c) {
  return __builtin_amdgcn_mfma_f32_16x16x16bf16_1k(__builtin_bit_cast(s16x4, a), __builtin_bit_cast(s16x4, b), c, 0, 0, 0);
}
__device__ __forceinline__ v2u lds_tr(lptr p) {
  return __builtin_bit_cast(v2u, __builtin_amdgcn_ds_read_tr16_b64_v4i16((LAS s16x4*)p));
}
template <class T> __device__ __forceinline__ T lds_ld(lptr p) { return *(LAS T*)p; }
template <class T> __device__ __forceinline__ void lds_st(lptr p, T v) { *(LAS T*)p = v; }
__device__ __forceinline__ v4u ldw(const u16* p) {
  if (W_NT) return __builtin_nontemporal_load((const GAS v4u*)p);
  return *(const GAS v4u*)p;
}
#define WAIT_VM(n) asm volatile("s_waitcnt vmcnt(%0)" ::"i"(n) : "memory")
#define WAIT_LGKM0() asm volatile("s_waitcnt lgkmcnt(0)" ::: "memory")

#define STAMP(idx)
#define SSTAMP(idx)
#define SSTAMP_DECL
#define SSTAMP_FLUSH()

__device__ __forceinline__ void flag_add(lptr L, int f) {
  WAIT_LGKM0();
  if ((threadIdx.x & 63) == 0) __hip_atomic_fetch_add((LAS unsigned*)(L + L_FLAG) + f, 1u, __ATOMIC_RELAXED, __HIP_MEMORY_SCOPE_WORKGROUP);
}
__device__ __forceinline__ void flag_wait(lptr L, int f, unsigned want) {
  volatile LAS unsigned* fl = (volatile LAS unsigned*)(L + L_FLAG);
#pragma nounroll
  for (unsigned sp = 0; fl[f] < want; ++sp) {
    if (sp > SPIN_LDS || fl[FL_ABORT]) { fl[FL_ABORT] = 1; break; }
    __builtin_amdgcn_s_sleep(1);
  }
  asm volatile("" ::: "memory");
}
__device__ __forceinline__ void chain_sync(lptr L, unsigned& epoch) {
  epoch += 4;
  flag_add(L, FL_SYNC);
  flag_wait(L, FL_SYNC, epoch);
}

struct Tag { unsigned a, b; };
__device__ __forceinline__ void put(__amdgpu_buffer_rsrc_t rs, int off, unsigned lo, unsigned hi, Tag t) {
  v4u g = {lo, t.a, hi, t.b};
  __builtin_amdgcn_raw_buffer_store_b128(g, rs, off, 0, 16);
}
template <int NL, int CAN = 0>
__device__ __forceinline__ void sweep(lptr L, __amdgpu_buffer_rsrc_t rs, const int (&off)[NL], v4u (&v)[NL], Tag t) {
  volatile LAS unsigned* fl = (volatile LAS unsigned*)(L + L_FLAG);
  if (CAN > 0) {
    const int oc = __builtin_amdgcn_readlane(off[NL - 1], 63);
#pragma nounroll
    for (unsigned sp = 0;; ++sp) {
      const v4u c = __builtin_amdgcn_raw_buffer_load_b128(rs, 0, oc, 16);
      if (__all((c.y == t.a) & (c.w == t.b))) break;
      if (sp > SPIN_GLOBAL || fl[FL_ABORT]) { fl[FL_ABORT] = 1; break; }
      __builtin_amdgcn_s_sleep(CAN);
      asm volatile("" ::: "memory");
    }
  }
#pragma nounroll
  for (unsigned sp = 0;; ++sp) {
#pragma unroll
    for (int k = 0; k < NL; ++k) v[k] = __builtin_amdgcn_raw_buffer_load_b128(rs, off[k], 0, 16);
    bool ok = true;
#pragma unroll
    for (int k = 0; k < NL; ++k) ok &= (v[k].y == t.a) & (v[k].w == t.b);
    if (__all(ok)) break;
    if (sp > SPIN_GLOBAL || fl[FL_ABORT]) { fl[FL_ABORT] = 1; break; }
    asm volatile("" ::: "memory");
  }
}
__device__ __forceinline__ f32x4 gval(v4u g) { return f32x4{bf_lo(g.x), bf_hi(g.x), bf_lo(g.z), bf_hi(g.z)}; }

struct Delta { unsigned m[4]; };
__device__ __forceinline__ Delta make_delta(int lane) {
  const int j = lane & 7;
  Delta d;
#pragma unroll
  for (int r = 0; r < 4; ++r) d.m[r] = (r == (j >> 1)) ? ((j & 1) ? 0xffff0000u : 0x0000ffffu) : 0u;
  return d;
}
__device__ __forceinline__ v4u delta_op(const Delta& d, unsigned pair, int hi) {
  const unsigned dup = __builtin_amdgcn_perm(pair, pair, hi ? 0x03020302u : 0x01000100u);
  return v4u{dup & d.m[0], dup & d.m[1], dup & d.m[2], dup & d.m[3]};
}

#define DMA_TILE(o0, o1, o2, o3, base, ldsaddr, AUXS)                                                    \
  do {                                                                                                   \
    unsigned keep_;                                                                                      \
    asm volatile("s_nop 4\n\ts_mov_b32 %0, m0\n\ts_mov_b32 m0, %6\n\ts_nop 0\n\t"                        \
                 "global_load_lds_dwordx4 %1, %5" AUXS "\n\t"                                            \
                 "global_load_lds_dwordx4 %2, %5 offset:1024" AUXS "\n\t"                                \
                 "global_load_lds_dwordx4 %3, %5 offset:2048" AUXS "\n\t"                                \
                 "global_load_lds_dwordx4 %4, %5 offset:3072" AUXS "\n\t"                                \
                 "s_mov_b32 m0, %0"                                                                      \
                 : "=&s"(keep_)                                                                          \
                 : "v"(o0), "v"(o1), "v"(o2), "v"(o3), "s"(base), "s"(ldsaddr)                           \
                 : "memory");                                                                            \
  } while (0)

constexpr int last_issued(int t, int tlast) {
  int li = 7;
  for (int s = 0; s <= t; ++s) {
    const int want = s + 8 - ((8 - LOOKA) * (s + 1 < SHRINK ? s + 1 : SHRINK)) / SHRINK;
    if (want > li) li += 1;
  }
  return li < tlast ? li : tlast;
}
template <bool HASW1, int PW, bool GATED>
__device__ __forceinline__ void stream_wave(const Params& p, lptr L, int x, int i, int w, int lane) {
  constexpr int TLAST = HASW1 ? 35 : 31;
  SSTAMP_DECL;
  const int g = lane >> 4, n = lane & 15;
  const size_t key0 = (size_t)x * SK + 1024 * i + 256 * w;
  const char* Kb = (const char*)p.K + key0 * 256;
  const char* Vb = (const char*)p.V + key0 * 256;
  const int jj = 16 * x + (i - 16);
  const char* W1b = (const char*)p.w1 + (size_t)(256 * w) * (FF * 2) + jj * 64;
  const unsigned ring = (unsigned)(size_t)L + L_RING + w * 32768;
  unsigned voK[4], voV[2], voW[4];
#pragma unroll
  for (int v = 0; v < 4; ++v) voK[v] = g * 256 + ((n ^ (4 * v + g)) << 4);
#pragma unroll
  for (int v = 0; v < 2; ++v) voV[v] = g * 256 + ((n ^ (((4 * v + g) & 7) << 1)) << 4);
#pragma unroll
  for (int v = 0; v < 4; ++v) voW[v] = (16 * v + (lane >> 2)) * (FF * 2) + (((lane & 3) ^ (((lane >> 4) & 1) << 1)) << 4) - v * 1024;

#define ISSUE(t)                                                                                                     \
  do {                                                                                                               \
    const unsigned la_ = ring + ((t)&7) * 4096;                                                                      \
    if ((t) < 16) { const char* b_ = Kb + (t)*4096; DMA_TILE(voK[0], voK[1], voK[2], voK[3], b_, la_, KV_AUX); }      \
    else if ((t) < 32) { const char* b_ = Vb + ((t)-16) * 4096; DMA_TILE(voV[0], voV[1], voV[0], voV[1], b_, la_, KV_AUX); } \
    else { const char* b_ = W1b + (size_t)((t)-32) * 64 * (FF * 2); DMA_TILE(voW[0], voW[1], voW[2], voW[3], b_, la_, W1_AUX); } \
  } while (0)

  if (GATED) flag_wait(L, FL_GO, 4);
  SSTAMP(7);
#pragma unroll
  for (int t = 0; t < PW; ++t) ISSUE(t);
#pragma unroll
  for (int t = PW; t < 8; ++t) { WAIT_VM(4 * (PW - 1)); ISSUE(t); }
  SSTAMP(1);
  flag_wait(L, FL_Q, 4);
  SSTAMP(2);

  v4u qf[4];
#pragma unroll
  for (int t4 = 0; t4 < 4; ++t4) qf[t4] = lds_ld<v4u>(L + L_Q + (n & 7) * 256 + t4 * 64 + g * 16);
  lptr rb = L + L_RING + w * 32768;
  int ka[4];
#pragma unroll
  for (int t4 = 0; t4 < 4; ++t4) ka[t4] = n * 256 + (((4 * t4 + g) ^ n) << 4);

  f32x4 sc[16];
#pragma unroll
  for (int t = 0; t < 16; ++t) {
    WAIT_VM(4 * (last_issued(t - 1, TLAST) - t));
    v4u kf[4];
#pragma unroll
    for (int t4 = 0; t4 < 4; ++t4) kf[t4] = lds_ld<v4u>(rb + (t & 7) * 4096 + ka[t4]);
    WAIT_LGKM0();
    if (last_issued(t, TLAST) > last_issued(t - 1, TLAST)) ISSUE(last_issued(t, TLAST));
    f32x4 a = {0.f, 0.f, 0.f, 0.f};
#pragma unroll
    for (int t4 = 0; t4 < 4; ++t4) a = mfma32(kf[t4], qf[t4], a);
    sc[t] = a;
  }
  SSTAMP(3);
  float m = -INFINITY;
#pragma unroll
  for (int t = 0; t < 16; ++t) m = fmaxf(fmaxf(fmaxf(sc[t][0], sc[t][1]), fmaxf(sc[t][2], sc[t][3])), m);
  m = max_16_32(m);
  const float mm = m * SC_LOG2;
  float lsum = 0.f;
  v2u pb[16];
#pragma unroll
  for (int t = 0; t < 16; ++t) {
    float e[4];
#pragma unroll
    for (int r = 0; r < 4; ++r) { e[r] = __builtin_amdgcn_exp2f(fmaf(sc[t][r], SC_LOG2, -mm)); lsum += e[r]; }
    pb[t] = v2u{pack2(e[0], e[1]), pack2(e[2], e[3])};
  }
  lsum = sum_16_32(lsum);
  if (lane < 8) lds_st<v2u>(L + L_STAT + (w * 8 + lane) * 8, v2u{__float_as_uint(mm), __float_as_uint(lsum)});
  flag_add(L, FL_STAT);

  int va[8];
  {
    const int q = (lane & 15) >> 2, pp = lane & 3, row = 4 * g + q;
#pragma unroll
    for (int u = 0; u < 8; ++u) va[u] = row * 256 + (((2 * u + (pp >> 1)) ^ ((row & 7) << 1)) << 4) + (pp & 1) * 8;
  }
  f32x4 o[8];
#pragma unroll
  for (int u = 0; u < 8; ++u) o[u] = f32x4{0.f, 0.f, 0.f, 0.f};
#pragma unroll
  for (int t = 16; t < 32; ++t) {
    WAIT_VM(4 * (last_issued(t - 1, TLAST) - t));
    v2u vf[8];
#pragma unroll
    for (int u = 0; u < 8; ++u) vf[u] = lds_tr(rb + (t & 7) * 4096 + va[u]);
    WAIT_LGKM0();
    if (last_issued(t, TLAST) > last_issued(t - 1, TLAST)) ISSUE(last_issued(t, TLAST));
    if (last_issued(t, 99) == 31 && last_issued(t - 1, 99) == 30) flag_add(L, FL_LASTV);
#pragma unroll
    for (int u = 0; u < 8; ++u) o[u] = mfma16(vf[u], pb[t - 16], o[u]);
  }
  SSTAMP(4);
  flag_wait(L, FL_FAC, 1);
  const float f = lds_ld<float>(L + L_FAC + (w * 8 + (n & 7)) * 4);
  if (n < 8) {
#pragma unroll
    for (int u = 0; u < 8; ++u) lds_st<f32x4>(rb + 4 * 4096 + n * 528 + (16 * u + 4 * g) * 4, o[u] * f);
  }
  flag_add(L, FL_ODONE);
  SSTAMP(5);
  WAIT_VM(0);
  SSTAMP(6);
  SSTAMP_FLUSH();
#undef ISSUE
}

__device__ __forceinline__ f32x4 red4(lptr L, int lane) {
  f32x4 s = lds_ld<f32x4>(L + L_RED + lane * 16);
#pragma unroll
  for (int ww = 1; ww < 4; ++ww) s += lds_ld<f32x4>(L + L_RED + ww * 1024 + lane * 16);
  return s;
}

__device__ __forceinline__ f32x4 gemv_tile128(lptr L, const v4u (&W)[16], const Delta& dl, int cw, int lane) {
  const int g = lane >> 4, b = (lane >> 3) & 1;
  const v4u a0 = lds_ld<v4u>(L + L_ACT + ((b * 4 + cw) * 4 + g) * 32), a1 = lds_ld<v4u>(L + L_ACT + ((b * 4 + cw) * 4 + g) * 32 + 16);
  f32x4 acc = {0.f, 0.f, 0.f, 0.f};
#pragma unroll
  for (int u = 0; u < 8; ++u) acc = mfma32(delta_op(dl, a0[u >> 1], u & 1), W[u], acc);
#pragma unroll
  for (int u = 0; u < 8; ++u) acc = mfma32(delta_op(dl, a1[u >> 1], u & 1), W[8 + u], acc);
  return acc;
}
__device__ __forceinline__ void gemv_rows32(const v4u (&W)[16], const Delta& dl, v4u av, f32x4& acc0, f32x4& acc1) {
  acc0 = f32x4{0.f, 0.f, 0.f, 0.f}; acc1 = acc0;
#pragma unroll
  for (int u = 0; u < 8; ++u) {
    const v4u a = delta_op(dl, av[u >> 1], u & 1);
    acc0 = mfma32(a, W[u], acc0);
    acc1 = mfma32(a, W[8 + u], acc1);
  }
}
__device__ __forceinline__ void load_rows32(v4u (&W)[16], const u16* Wg, int row0, int cw, int lane) {
  const int g = lane >> 4, c = lane & 15;
#pragma unroll
  for (int cb = 0; cb < 2; ++cb)
#pragma unroll
    for (int u = 0; u < 8; ++u) W[cb * 8 + u] = ldw(Wg + (size_t)(row0 + 4 * u + g) * D + (2 * cw + cb) * 128 + 8 * c);
  __builtin_amdgcn_sched_barrier(0);
}
__device__ __forceinline__ void load_tile128(v4u (&W)[16], const u16* Wg, int ld, int row0, int col0, int cw, int lane) {
  const int g = lane >> 4, c = lane & 15;
#pragma unroll
  for (int u = 0; u < 16; ++u) W[u] = ldw(Wg + (size_t)(row0 + 64 * cw + 4 * u + g) * ld + col0 + 8 * c);
  __builtin_amdgcn_sched_barrier(0);
}
__device__ __forceinline__ void put_owner(__amdgpu_buffer_rsrc_t rs, int base, int prod, int cw, int lane, f32x4 acc0, f32x4 acc1, Tag tg) {
  const int g = lane >> 4, c = lane & 15;
  put(rs, base + (((32 * cw + c) * 128 + prod) * 4 + g) * 16, pack2(acc0[0], acc0[1]), pack2(acc0[2], acc0[3]), tg);
  put(rs, base + (((32 * cw + 16 + c) * 128 + prod) * 4 + g) * 16, pack2(acc1[0], acc1[1]), pack2(acc1[2], acc1[3]), tg);
}
template <int NP>
__device__ __forceinline__ f32x4 gather_owner(lptr L, __amdgpu_buffer_rsrc_t rs, int base, int owner, int cw, int lane, Tag tg) {
  const int t = cw * 64 + lane;
  int off[2]; v4u v[2];
  const int p1 = (t >> 2) + 64;
  off[0] = base + ((owner * 128 + (t >> 2)) * 4 + (t & 3)) * 16;
  off[1] = p1 < NP ? base + ((owner * 128 + p1) * 4 + (t & 3)) * 16 : off[0];
  sweep<2, CAN_TAIL>(L, rs, off, v, tg);
  f32x4 s = gval(v[0]);
  if (p1 < NP) s += gval(v[1]);
#pragma unroll
  for (int e = 0; e < 4; ++e) { float a = s[e]; a += dpp<ROR4>(a); a += dpp<ROR8>(a); s[e] = sum_16_32(a); }
  if (lane < 4) lds_st<f32x4>(L + L_RED + (cw * 4 + lane) * 16, s);
  WAIT_LGKM0();
  __builtin_amdgcn_s_barrier();
  f32x4 r = lds_ld<f32x4>(L + L_RED + (lane & 3) * 16);
#pragma unroll
  for (int ww = 1; ww < 4; ++ww) r += lds_ld<f32x4>(L + L_RED + (ww * 4 + (lane & 3)) * 16);
  return r;
}
__device__ __forceinline__ float silu(float v) { return v / (1.0f + __expf(-v)); }
__device__ __forceinline__ float gelu_tanh(float a) {
  const float u = 0.7978845608028654f * (a + 0.044715f * a * a * a);
  const float e = __expf(2.0f * u);
  const float th = 1.0f - 2.0f / (e + 1.0f);
  return 0.5f * a * (1.0f + th);
}

__device__ __forceinline__ void gather_q(lptr L, __amdgpu_buffer_rsrc_t rs, int x, int ct, Tag tg) {
  int off[4]; v4u v[4];
#pragma unroll
  for (int k = 0; k < 4; ++k) off[k] = WS_F2 + ((k * 2 + (x >> 2)) * 1024 + (x & 3) * 256 + ct) * 16;
  sweep<4, CAN_FRONT>(L, rs, off, v, tg);
  const f32x4 s = (gval(v[0]) + gval(v[1])) + (gval(v[2]) + gval(v[3]));
  lds_st<v2u>(L + L_Q + ct * 8, v2u{pack2(s[0], s[1]), pack2(s[2], s[3])});
  flag_add(L, FL_Q);
}
__device__ __forceinline__ void stats_exchange(lptr L, __amdgpu_buffer_rsrc_t rs, int x, int i, int lane, Tag tg) {
  flag_wait(L, FL_STAT, 4);
  const int h = lane & 7;
  float mw[4], lw[4];
#pragma unroll
  for (int w = 0; w < 4; ++w) { const v2u s = lds_ld<v2u>(L + L_STAT + (w * 8 + h) * 8); mw[w] = __uint_as_float(s.x); lw[w] = __uint_as_float(s.y); }
  const float mc = fmaxf(fmaxf(mw[0], mw[1]), fmaxf(mw[2], mw[3]));
  float lc = 0.f;
#pragma unroll
  for (int w = 0; w < 4; ++w) lc += lw[w] * __builtin_amdgcn_exp2f(mw[w] - mc);
  if (lane < 8) put(rs, WS_ST + ((x * 32 + i) * 8 + h) * 16, __float_as_uint(mc), __float_as_uint(lc), tg);
  int off[4]; v4u v[4];
#pragma unroll
  for (int k = 0; k < 4; ++k) off[k] = WS_ST + ((x * 32 + (lane >> 3) + 8 * k) * 8 + h) * 16;
  sweep<4, CAN_SLOW>(L, rs, off, v, tg);
  float M = -INFINITY;
#pragma unroll
  for (int k = 0; k < 4; ++k) M = fmaxf(M, __uint_as_float(v[k].x));
  M = fmaxf(M, dpp<ROR8>(M));
  M = max_16_32(M);
  float S = 0.f;
#pragma unroll
  for (int k = 0; k < 4; ++k) S += __uint_as_float(v[k].z) * __builtin_amdgcn_exp2f(__uint_as_float(v[k].x) - M);
  S += dpp<ROR8>(S);
  S = sum_16_32(S);
  const float inv = 1.0f / S;
  if (lane < 32) {
    const int w = lane >> 3;
    const float mws = w == 0 ? mw[0] : w == 1 ? mw[1] : w == 2 ? mw[2] : mw[3];
    lds_st<float>(L + L_FAC + lane * 4, __builtin_amdgcn_exp2f(mws - M) * inv);
  }
  flag_add(L, FL_FAC);
}
__device__ __forceinline__ void publish_partial(lptr L, __amdgpu_buffer_rsrc_t rs, int x, int i, int ct, Tag tg) {
  flag_wait(L, FL_ODONE, 4);
  const int h = ct >> 5, gi = ct & 31;
  f32x4 s = lds_ld<f32x4>(L + L_RING + 4 * 4096 + h * 528 + gi * 16);
#pragma unroll
  for (int w = 1; w < 4; ++w) s += lds_ld<f32x4>(L + L_RING + w * 32768 + 4 * 4096 + h * 528 + gi * 16);
  put(rs, WS_H1 + (((((x >> 2) * 32 + (x & 3) * 8 + h) * 4 + (gi >> 3)) * 32 + i) * 8 + (gi & 7)) * 16, pack2(s[0], s[1]), pack2(s[2], s[3]), tg);
}

__device__ __forceinline__ void chain_a(const Params& p, lptr L, __amdgpu_buffer_rsrc_t rs, int x, int i, int cw, int lane, Tag tg) {
  const int a = 16 * x + i, ct = cw * 64 + lane, g = lane >> 4;
  const Delta dl = make_delta(lane);
  unsigned sync = 0;
  const int cg = a >> 2, ks = a & 3, row0 = 256 * ks + 64 * cw;
  v4u W[16];
  unsigned cpair = *(const GAS unsigned*)(p.c + (lane >> 5) * D + row0 + 2 * (lane & 31));
  v2u xown = *(const GAS v2u*)(p.x + ((lane >> 1) & 1) * D + 8 * a + 4 * (lane & 1));
  v2u b2own = *(const GAS v2u*)(p.b2 + 8 * a + 4 * (lane & 1));
  __builtin_amdgcn_sched_barrier(0);
#pragma unroll
  for (int u = 0; u < 8; ++u) W[u] = ldw(p.wmod + (size_t)(row0 + 8 * u + (lane >> 3)) * NMOD + 64 * cg + 8 * (lane & 7));
  __builtin_amdgcn_sched_barrier(0);
  asm volatile("" : "+v"(cpair), "+v"(xown), "+v"(b2own), "+v"(tg.a));
  tg.a += 1u; tg.b = ~tg.a;
  if (!GATEA_Q) { asm volatile("" : "+v"(W[GOA])); flag_add(L, FL_GO); }
  {
    const int rr = 2 * (lane & 31), b = lane >> 5;
    lds_st<unsigned>(L + L_ACT + cw * 256 + (b * 64 + ((rr >> 1) & 3) * 16 + (rr >> 3) * 2) * 2, pack2(silu(bf_lo(cpair)), silu(bf_hi(cpair))));
    const int bb = (lane >> 3) & 1;
    const v4u a0 = lds_ld<v4u>(L + L_ACT + cw * 256 + (bb * 64 + g * 16) * 2), a1 = lds_ld<v4u>(L + L_ACT + cw * 256 + (bb * 64 + g * 16) * 2 + 16);
    f32x4 acc0 = {0.f, 0.f, 0.f, 0.f}, acc1 = acc0;
#pragma unroll
    for (int u = 0; u < 8; ++u) {
      const unsigned pr = u < 4 ? a0[u] : a1[u - 4];
      acc0 = mfma32(delta_op(dl, pr, 0), W[u], acc0);
      acc1 = mfma32(delta_op(dl, pr, 1), W[u], acc1);
    }
    f32x4 t = (lane & 8) ? acc1 : acc0;
#pragma unroll
    for (int e = 0; e < 4; ++e) t[e] += dpp<ROR8>(t[e]);
    lds_st<f32x4>(L + L_RED + cw * 1024 + lane * 16, t);
    chain_sync(L, sync);
    if (cw == 0 && (lane & 8) == 0) {
      const f32x4 s = red4(L, lane);
      put(rs, WS_F1 + ((ks * 2 + (g >> 1)) * 512 + 16 * cg + 2 * (lane & 7) + (g & 1)) * 16, pack2(s[0], s[1]), pack2(s[2], s[3]), tg);
    }
  }
  STAMP(1);
  if (!WO_LATE) load_rows32(W, p.wo, 32 * a, cw, lane);
  gather_q(L, rs, x, ct, tg);
  if (GATEA_Q) flag_add(L, FL_GO);
  if (WO_LATE) load_rows32(W, p.wo, 32 * a, cw, lane);
  STAMP(2);
  if (cw == 0) {
    const int v = (lane >> 2) & 3, b = (lane >> 1) & 1, half = lane & 1;
    int off[1] = {WS_F3A + (((lane >> 4) * 2 + b) * 1024 + v * 256 + 2 * a + half) * 16};
    v4u gv[1];
    sweep<1, CAN_SLOW>(L, rs, off, gv, tg);
    f32x4 s = gval(gv[0]);
#pragma unroll
    for (int e = 0; e < 4; ++e) s[e] = sum_16_32(s[e]);
    const unsigned lo = pack2(s[0], s[1]), hi = pack2(s[2], s[3]);
    if (lane < 16) {
      if (v == 1 || v == 2) put(rs, WS_F3B + ((b * 2 + v - 1) * 256 + 2 * a + half) * 16, lo, hi, tg);
      else lds_st<f32x4>(L + L_GAB + ((v ? 1 : 0) * 4 + b * 2 + half) * 16, f32x4{bf_lo(lo), bf_hi(lo), bf_lo(hi), bf_hi(hi)});
    }
    STAMP(3);
    stats_exchange(L, rs, x, i, lane, tg);
  }
  STAMP(4);
  publish_partial(L, rs, x, i, ct, tg);
  STAMP(5);
  {
    const int b = ct >> 7, sl = (ct & 127) >> 3, gi = ct & 7;
    int off[2]; v4u v[2];
#pragma unroll
    for (int k = 0; k < 2; ++k) off[k] = WS_H1 + ((((b * 32 + (a >> 2)) * 4 + (a & 3)) * 32 + sl + 16 * k) * 8 + gi) * 16;
    sweep<2, CAN_TAIL>(L, rs, off, v, tg);
    STAMP(6);
    f32x4 s = gval(v[0]) + gval(v[1]);
#pragma unroll
    for (int e = 0; e < 4; ++e) { float t = s[e]; t += dpp<ROR8>(t); s[e] = sum_16_32(t); }
    if (lane < 8) {
#pragma unroll
      for (int e = 0; e < 4; ++e) lds_st<float>(L + L_RED + ((cw * 4 + e) * 8 + lane) * 4, s[e]);
    }
    WAIT_LGKM0();
    __builtin_amdgcn_s_barrier();
    const int bb = (lane >> 3) & 1;
    lptr r0 = L + L_RED + ((2 * bb * 4 + g) * 8) * 4, r1 = r0 + 128;
    const f32x4 o0 = lds_ld<f32x4>(r0) + lds_ld<f32x4>(r1), o1 = lds_ld<f32x4>(r0 + 16) + lds_ld<f32x4>(r1 + 16);
    const v4u av = {pack2(o0[0], o0[1]), pack2(o0[2], o0[3]), pack2(o1[0], o1[1]), pack2(o1[2], o1[3])};
    f32x4 acc0, acc1;
    gemv_rows32(W, dl, av, acc0, acc1);
    put_owner(rs, WS_H2, a, cw, lane, acc0, acc1, tg);
    WAIT_LGKM0();
    __builtin_amdgcn_s_barrier();
  }
  STAMP(7);
  const f32x4 attn = gather_owner<128>(L, rs, WS_H2, a, cw, lane, tg);
  STAMP(8);
  const f32x4 ga = lds_ld<f32x4>(L + L_GAB + (lane & 3) * 16), gb = lds_ld<f32x4>(L + L_GAB + (4 + (lane & 3)) * 16);
  const f32x4 xv = {bf_lo(xown.x), bf_hi(xown.x), bf_lo(xown.y), bf_hi(xown.y)};
  f32x4 x1;
#pragma unroll
  for (int e = 0; e < 4; ++e) x1[e] = xv[e] + ga[e] * rbf(attn[e]);
  const unsigned x1lo = pack2(x1[0], x1[1]), x1hi = pack2(x1[2], x1[3]);
  if (cw == 0 && lane < 4 * NREP3) put(rs, WS_H3 + (((lane >> 2) * 128 + a) * 4 + (lane & 3)) * 16, x1lo, x1hi, tg);
  STAMP(9);
  WAIT_LGKM0();
  __builtin_amdgcn_s_barrier();
  const f32x4 mlp = gather_owner<112>(L, rs, WS_H4, a, cw, lane, tg);
  STAMP(10);
  if (cw == 0 && lane < 4) {
    const f32x4 x1r = {bf_lo(x1lo), bf_hi(x1lo), bf_lo(x1hi), bf_hi(x1hi)};
    const f32x4 bv = {bf_lo(b2own.x), bf_hi(b2own.x), bf_lo(b2own.y), bf_hi(b2own.y)};
    f32x4 r;
#pragma unroll
    for (int e = 0; e < 4; ++e) r[e] = x1r[e] + gb[e] * (mlp[e] + bv[e]);
    if (*(volatile LAS unsigned*)(L + L_FLAG + FL_ABORT * 4)) r = f32x4{NAN, NAN, NAN, NAN};
    *(GAS v2u*)(p.out + (lane >> 1) * D + 8 * a + 4 * (lane & 1)) = v2u{pack2(r[0], r[1]), pack2(r[2], r[3])};
    if (a == 0 && lane == 0) __hip_atomic_store((GAS unsigned*)(p.ws + WS_EPOCH), tg.a, __ATOMIC_RELAXED, __HIP_MEMORY_SCOPE_AGENT);
  }
  STAMP(11);
}

__device__ __forceinline__ void ln_rows(f32x4 (&v)[4]) {
  float s = 0.f;
#pragma unroll
  for (int k = 0; k < 4; ++k) s += (v[k][0] + v[k][1]) + (v[k][2] + v[k][3]);
  const float mean = wave_sum(s) * (1.0f / D);
  float q = 0.f;
#pragma unroll
  for (int k = 0; k < 4; ++k) { v[k] -= mean; q += (v[k][0] * v[k][0] + v[k][1] * v[k][1]) + (v[k][2] * v[k][2] + v[k][3] * v[k][3]); }
  const float r = 1.0f / sqrtf(wave_sum(q) * (1.0f / D) + LN_EPS);
#pragma unroll
  for (int k = 0; k < 4; ++k) v[k] *= r;
}
__device__ __forceinline__ void chain_b(const Params& p, lptr L, __amdgpu_buffer_rsrc_t rs, int x, int i, int cw, int lane, Tag tg) {
  const int j = 16 * x + (i - 16), ct = cw * 64 + lane, g = lane >> 4;
  const Delta dl = make_delta(lane);
  unsigned sync = 0;
  const int ks = x & 3, cgrp = (x >> 2) * 16 + (i - 16);
  v4u W[16];
  unsigned cpair = *(const GAS unsigned*)(p.c + (lane >> 5) * D + 256 * ks + 64 * cw + 2 * (lane & 31));
  unsigned b1u = *(const GAS u16*)(p.b1 + 32 * (j < 112 ? j : 0) + (lane & 31));
  v4u xa = *(const GAS v4u*)(p.x + (cw & 1) * D + 16 * lane), xb = *(const GAS v4u*)(p.x + (cw & 1) * D + 16 * lane + 8);
  v2u xs = *(const GAS v2u*)(p.x + (cw & 1) * D + 256 * ks + 4 * lane);
  __builtin_amdgcn_sched_barrier(0);
  load_tile128(W, p.wq, 4096, 256 * ks, 128 * cgrp, cw, lane);
  asm volatile("" : "+v"(cpair), "+v"(b1u), "+v"(xa), "+v"(xb), "+v"(xs), "+v"(tg.a));
  tg.a += 1u; tg.b = ~tg.a;
  if (cw < 2) {
    const int b = cw;
    float xv[16];
#pragma unroll
    for (int e = 0; e < 4; ++e) { xv[2 * e] = bf_lo(xa[e]); xv[2 * e + 1] = bf_hi(xa[e]); xv[8 + 2 * e] = bf_lo(xb[e]); xv[9 + 2 * e] = bf_hi(xb[e]); }
    float s = 0.f;
#pragma unroll
    for (int e = 0; e < 16; ++e) s += xv[e];
    const float mean = wave_sum(s) * (1.0f / D);
    float q = 0.f;
#pragma unroll
    for (int e = 0; e < 16; ++e) { const float d = xv[e] - mean; q += d * d; }
    const float rstd = 1.0f / sqrtf(wave_sum(q) * (1.0f / D) + LN_EPS);
    if (!GATEB_Q) { asm volatile("" : "+v"(W[GOB])); flag_add(L, FL_GO); }
    int off[8]; v4u v[8];
#pragma unroll
    for (int k = 0; k < 8; ++k) off[k] = WS_F1 + (((k >> 1) * 2 + b) * 512 + (k & 1) * 256 + 64 * ks + lane) * 16;
    sweep<8, CAN_FRONT>(L, rs, off, v, tg);
    const f32x4 sa = (gval(v[0]) + gval(v[2])) + (gval(v[4]) + gval(v[6])), ca = (gval(v[1]) + gval(v[3])) + (gval(v[5]) + gval(v[7]));
    const float xs4[4] = {bf_lo(xs.x), bf_hi(xs.x), bf_lo(xs.y), bf_hi(xs.y)};
#pragma unroll
    for (int e = 0; e < 4; ++e) {
      const float h = (xs4[e] - mean) * rstd * (1.0f + rbf(ca[e])) + rbf(sa[e]);
      lds_st<u16>(L + L_ACT + (((b * 4 + (lane >> 4)) * 4 + e) * 16 + (lane & 15)) * 2, (u16)pack2(h, h));
    }
  } else if (!GATEB_Q) {
    asm volatile("" : "+v"(W[GOB]));
    flag_add(L, FL_GO);
  }
  chain_sync(L, sync);
  STAMP(1);
  {
    const f32x4 acc = gemv_tile128(L, W, dl, cw, lane);
    lds_st<f32x4>(L + L_RED + cw * 1024 + lane * 16, acc);
    chain_sync(L, sync);
    if (cw == 0) {
      const f32x4 s = red4(L, lane);
      put(rs, WS_F2 + ((ks * 2 + (g >> 1)) * 1024 + 32 * cgrp + 2 * (lane & 15) + (g & 1)) * 16, pack2(s[0], s[1]), pack2(s[2], s[3]), tg);
    }
  }
  STAMP(2);
  gather_q(L, rs, x, ct, tg);
  if (GATEB_Q) flag_add(L, FL_GO);
  STAMP(3);
  load_tile128(W, p.wmod, NMOD, 256 * ks, 2048 + 128 * cgrp, cw, lane);
  {
    const int rr = 2 * (lane & 31), b = lane >> 5;
    lptr ap = L + L_ACT + (((b * 4 + cw) * 4 + (rr & 3)) * 16 + (rr >> 2)) * 2;
    const unsigned sp = pack2(silu(bf_lo(cpair)), silu(bf_hi(cpair)));
    chain_sync(L, sync);
    lds_st<u16>(ap, (u16)sp);
    lds_st<u16>(ap + 32, (u16)(sp >> 16));
    const f32x4 acc = gemv_tile128(L, W, dl, cw, lane);
    lds_st<f32x4>(L + L_RED + cw * 1024 + lane * 16, acc);
    chain_sync(L, sync);
    if (cw == 0) {
      const f32x4 s = red4(L, lane);
      put(rs, WS_F3A + ((ks * 2 + (g >> 1)) * 1024 + 32 * cgrp + 2 * (lane & 15) + (g & 1)) * 16, pack2(s[0], s[1]), pack2(s[2], s[3]), tg);
      STAMP(4);
      stats_exchange(L, rs, x, i, lane, tg);
    }
  }
  STAMP(5);
  if (j < 112) {
    int off[4]; v4u v[4];
#pragma unroll
    for (int k = 0; k < 4; ++k) off[k] = WS_F3B + (k * 256 + ct) * 16;
    sweep<4, CAN_SLOW>(L, rs, off, v, tg);
#pragma unroll
    for (int k = 0; k < 4; ++k) lds_st<v2u>(L + L_SBCB + (k * 1024 + 4 * ct) * 2, v2u{v[k].x, v[k].z});
    flag_wait(L, FL_LASTV, 4);
    load_rows32(W, p.w2, 32 * j, cw, lane);
  }
  STAMP(6);
  publish_partial(L, rs, x, i, ct, tg);
  STAMP(7);
  if (j >= 112) return;
  if (cw < 2) {
    const int b = cw;
    int off[4]; v4u v[4];
#pragma unroll
    for (int k = 0; k < 4; ++k) off[k] = WS_H3 + ((x * 128 + (k >> 1) * 64 + lane) * 4 + 2 * b + (k & 1)) * 16;
    sweep<4, CAN_TAIL>(L, rs, off, v, tg);
    STAMP(8);
    f32x4 y[4];
#pragma unroll
    for (int k = 0; k < 4; ++k) y[k] = gval(v[k]);
    ln_rows(y);
#pragma unroll
    for (int k = 0; k < 4; ++k) {
      const int col = 8 * ((k >> 1) * 64 + lane) + 4 * (k & 1);
      const v2u sb = lds_ld<v2u>(L + L_SBCB + ((b * 2 + 0) * 1024 + col) * 2), cb = lds_ld<v2u>(L + L_SBCB + ((b * 2 + 1) * 1024 + col) * 2);
      y[k][0] = y[k][0] * (1.0f + bf_lo(cb.x)) + bf_lo(sb.x);
      y[k][1] = y[k][1] * (1.0f + bf_hi(cb.x)) + bf_hi(sb.x);
      y[k][2] = y[k][2] * (1.0f + bf_lo(cb.y)) + bf_lo(sb.y);
      y[k][3] = y[k][3] * (1.0f + bf_hi(cb.y)) + bf_hi(sb.y);
    }
    ln_rows(y);
#pragma unroll
    for (int k = 0; k < 4; ++k) {
      const int gq = 2 * ((k >> 1) * 64 + lane) + (k & 1);
      const int pos = (gq >> 3) * 32 + (gq & 3) * 8 + ((gq >> 2) & 1) * 4;
      lds_st<v2u>(L + L_Y + (b * D + pos) * 2, v2u{pack2(y[k][0], y[k][1]), pack2(y[k][2], y[k][3])});
    }
  }
  WAIT_LGKM0();
  __builtin_amdgcn_s_barrier();
  STAMP(9);
  {
    f32x4 acc[2] = {{0.f, 0.f, 0.f, 0.f}, {0.f, 0.f, 0.f, 0.f}};
    const int q = (lane & 15) >> 2, pp = lane & 3;
#pragma unroll
    for (int kk = 0; kk < 8; ++kk) {
      const v4u ya = lds_ld<v4u>(L + L_Y + ((lane & 1) * D + 256 * cw + 32 * kk + 8 * g) * 2);
#pragma unroll
      for (int ch = 0; ch < 2; ++ch) {
        v2u w[2];
#pragma unroll
        for (int hh = 0; hh < 2; ++hh) {
          const int row = (kk & 1) * 32 + 16 * hh + 4 * g + q;
          const int pc = (2 * ch + (pp >> 1)) ^ (((row >> 2) & 1) << 1);
          w[hh] = lds_tr(L + L_RING + cw * 32768 + (kk >> 1) * 4096 + row * 64 + pc * 16 + (pp & 1) * 8);
        }
        acc[ch] = mfma32(ya, v4u{w[0].x, w[0].y, w[1].x, w[1].y}, acc[ch]);
      }
    }
    if (lane < 16) {
#pragma unroll
      for (int ch = 0; ch < 2; ++ch)
#pragma unroll
        for (int b = 0; b < 2; ++b) lds_st<float>(L + L_RED + (cw * 64 + b * 32 + ch * 16 + lane) * 4, acc[ch][b]);
    }
    WAIT_LGKM0();
    __builtin_amdgcn_s_barrier();
    float a = bf_lo(b1u);
#pragma unroll
    for (int ww = 0; ww < 4; ++ww) a += lds_ld<float>(L + L_RED + (ww * 64 + lane) * 4);
    const float hv = gelu_tanh(rbf(a));
    const int k = lane & 31;
    lds_st<u16>(L + L_HID + cw * 128 + ((lane >> 5) * 32 + (k & 3) * 8 + (k >> 2)) * 2, (u16)pack2(hv, hv));
    const v4u av = lds_ld<v4u>(L + L_HID + cw * 128 + (((lane >> 3) & 1) * 32 + g * 8) * 2);
    f32x4 acc0, acc1;
    gemv_rows32(W, dl, av, acc0, acc1);
    put_owner(rs, WS_H4, j, cw, lane, acc0, acc1, tg);
  }
  STAMP(10);
}

__global__ void __launch_bounds__(512, 2) fused_decode_block(Params p) {
  extern __shared__ __attribute__((aligned(16))) unsigned char smem[];
  lptr L = (lptr)smem;
  const int tid = threadIdx.x, lane = tid & 63, wave = __builtin_amdgcn_readfirstlane(tid >> 6);
  const int x = blockIdx.x & 7, i = blockIdx.x >> 3;
  STAMP(0);
  if (tid < 16) *(LAS unsigned*)(L + L_FLAG + tid * 4) = 0u;
  WAIT_LGKM0();
  __builtin_amdgcn_s_barrier();
  if (wave < 4) {
    if (i < 16) stream_wave<false, PREW, GATE != 0 || GATEA_Q != 0>(p, L, x, i, wave, lane);
    else if (16 * x + (i - 16) < 112) stream_wave<true, PREWB, GATE != 0 || GATEB_Q != 0>(p, L, x, i, wave, lane);
    else stream_wave<false, PREWB, GATE != 0 || GATEB_Q != 0>(p, L, x, i, wave, lane);
    return;
  }
  __builtin_amdgcn_s_setprio(2);
  const __amdgpu_buffer_rsrc_t rs = __builtin_amdgcn_make_buffer_rsrc((void*)p.ws, 0, WS_END, 0x00020000);
  const Tag tg = {__hip_atomic_load((GAS unsigned*)(p.ws + WS_EPOCH), __ATOMIC_RELAXED, __HIP_MEMORY_SCOPE_AGENT), 0u};
  if (i < 16) chain_a(p, L, rs, x, i, wave - 4, lane, tg);
  else chain_b(p, L, rs, x, i, wave - 4, lane, tg);
}

}

extern "C" void kernel_launch(void* const* d_in, const int* in_sizes, int n_in, void* d_out, int out_size, void* d_ws, size_t ws_size,
                              hipStream_t stream) {
  Params p;
  p.x = (const u16*)d_in[0];
  p.K = (const u16*)d_in[1];
  p.V = (const u16*)d_in[2];
  p.wq = (const u16*)d_in[3];
  p.wo = (const u16*)d_in[4];
  p.w1 = (const u16*)d_in[6];
  p.b1 = (const u16*)d_in[7];
  p.w2 = (const u16*)d_in[8];
  p.b2 = (const u16*)d_in[9];
  p.c = (const u16*)d_in[10];
  p.wmod = (const u16*)d_in[11];
  p.out = (u16*)d_out;
  p.ws = (unsigned char*)d_ws;
  (void)hipFuncSetAttribute((const void*)fused_decode_block, hipFuncAttributeMaxDynamicSharedMemorySize, LDS_BYTES);
  hipLaunchKernelGGL(fused_decode_block, dim3(256), dim3(512), LDS_BYTES, stream, p);
}
```

```cpp
#define GATE 0
#define XUS 41
#include <hip/hip_runtime.h>
#include <hip/hip_bf16.h>
#include <stdint.h>

#define GAS __attribute__((address_space(1)))
#define LAS __attribute__((address_space(3)))
typedef unsigned v4u __attribute__((ext_vector_type(4)));
typedef unsigned v2u __attribute__((ext_vector_type(2)));
typedef float f32x4 __attribute__((ext_vector_type(4)));
typedef __bf16 bf16x8 __attribute__((ext_vector_type(8)));
typedef __bf16 bf16x2 __attribute__((ext_vector_type(2)));
typedef short s16x4 __attribute__((ext_vector_type(4)));
typedef unsigned short u16;
typedef LAS unsigned char* lptr;

#define STAMPS 0
#define PREW 2
#define LOOKA 8
#define SHRINK 16
#define CAN_FRONT 2
#define CAN_TAIL 1
#define CAN_SLOW 16
#define GOA 3
#define GOB 7
#define W1_AUX " nt"
#define PREWB PREW
#define GATEB_Q 0
#define GATEA_Q 0
#define WO_LATE 0
#define KV_AUX " nt"

namespace {

constexpr int D = 1024, SK = 32768, FF = 3584, NMOD = 6144;
constexpr float SC_LOG2 = 0.08838834764831845f * 1.4426950408889634f;
constexpr float LN_EPS = 1e-5f;

constexpr int WS_EPOCH = 0;
constexpr int WS_F1  = 4096;
constexpr int WS_F2  = WS_F1 + 4096 * 16;
constexpr int WS_F3A = WS_F2 + 8192 * 16;
constexpr int WS_F3B = WS_F3A + 8192 * 16;
constexpr int WS_ST  = WS_F3B + 1024 * 16;
constexpr int WS_H1  = WS_ST + 2048 * 16;
constexpr int WS_H2  = WS_H1 + 65536 * 16;
constexpr int NREP3  = 8;
constexpr int WS_H3  = WS_H2 + 65536 * 16;
constexpr int WS_H4  = WS_H3 + NREP3 * 512 * 16;
constexpr int WS_END = WS_H4 + 65536 * 16;
constexpr int WS_STAMP = 8 << 20;

constexpr int L_RING = 0;
constexpr int L_MISC = 131072;
constexpr int L_ACT  = L_MISC;
constexpr int L_Q    = L_ACT + 1024;
constexpr int L_RED  = L_Q + 2048;
constexpr int L_SBCB = L_RED + 4096;
constexpr int L_Y    = L_SBCB + 8192;
constexpr int L_STAT = L_Y + 4096;
constexpr int L_FAC  = L_STAT + 256;
constexpr int L_GAB  = L_FAC + 128;
constexpr int L_HID  = L_GAB + 128;
constexpr int L_FLAG = L_HID + 512;
constexpr int LDS_BYTES = 163840;
static_assert(L_FLAG + 64 <= LDS_BYTES, "LDS map");
enum { FL_SYNC = 0, FL_Q = 1, FL_STAT = 2, FL_FAC = 3, FL_ODONE = 4, FL_LASTV = 5, FL_ABORT = 6, FL_GO = 7 };

constexpr unsigned SPIN_GLOBAL = 1u << 17;
constexpr unsigned SPIN_LDS = 1u << 22;

struct Params {
  const u16 *x, *K, *V, *wq, *wo, *w1, *b1, *w2, *b2, *c, *wmod;
  u16* out;
  unsigned char* ws;
};

__device__ __forceinline__ float bf_lo(unsigned u) { return __builtin_bit_cast(float, u << 16); }
__device__ __forceinline__ float bf_hi(unsigned u) { return __builtin_bit_cast(float, u & 0xffff0000u); }
__device__ __forceinline__ unsigned pack2(float lo, float hi) { bf16x2 r = {(__bf16)lo, (__bf16)hi}; return __builtin_bit_cast(unsigned, r); }
__device__ __forceinline__ float rbf(float v) { return bf_lo(pack2(v, v) & 0xffffu); }
template <int CTRL> __device__ __forceinline__ float dpp(float v) {
  return __builtin_bit_cast(float, __builtin_amdgcn_mov_dpp(__builtin_bit_cast(int, v), CTRL, 0xf, 0xf, true));
}
constexpr int XOR1 = 0xB1, XOR2 = 0x4E, ROR4 = 0x124, ROR8 = 0x128;
__device__ __forceinline__ float sum_16_32(float v) {
  auto s = __builtin_amdgcn_permlane16_swap(__float_as_uint(v), __float_as_uint(v), false, false);
  v = __uint_as_float(s[0]) + __uint_as_float(s[1]);
  auto t = __builtin_amdgcn_permlane32_swap(__float_as_uint(v), __float_as_uint(v), false, false);
  return __uint_as_float(t[0]) + __uint_as_float(t[1]);
}
__device__ __forceinline__ float max_16_32(float v) {
  auto s = __builtin_amdgcn_permlane16_swap(__float_as_uint(v), __float_as_uint(v), false, false);
  v = fmaxf(__uint_as_float(s[0]), __uint_as_float(s[1]));
  auto t = __builtin_amdgcn_permlane32_swap(__float_as_uint(v), __float_as_uint(v), false, false);
  return fmaxf(__uint_as_float(t[0]), __uint_as_float(t[1]));
}
__device__ __forceinline__ float wave_sum(float v) {
  v += dpp<XOR1>(v); v += dpp<XOR2>(v); v += dpp<ROR4>(v); v += dpp<ROR8>(v);
  return sum_16_32(v);
}
__device__ __forceinline__ f32x4 mfma32(v4u a, v4u b, f32x4 c) {
  return __builtin_amdgcn_mfma_f32_16x16x32_bf16(__builtin_bit_cast(bf16x8, a), __builtin_bit_cast(bf16x8, b), c, 0, 0, 0);
}
__device__ __forceinline__ f32x4 mfma16(v2u a, v2u b, f32x4 c) {
  return __builtin_amdgcn_mfma_f32_16x16x16bf16_1k(__builtin_bit_cast(s16x4, a), __builtin_bit_cast(s16x4, b), c, 0, 0, 0);
}
__device__ __forceinline__ v2u lds_tr(lptr p) {
  return __builtin_bit_cast(v2u, __builtin_amdgcn_ds_read_tr16_b64_v4i16((LAS s16x4*)p));
}
template <class T> __device__ __forceinline__ T lds_ld(lptr p) { return *(LAS T*)p; }
template <class T> __device__ __forceinline__ void lds_st(lptr p, T v) { *(LAS T*)p = v; }
#define W_NT 1
__device__ __forceinline__ v4u ldw(const u16* p) {
  if (W_NT) return __builtin_nontemporal_load((const GAS v4u*)p);
  return *(const GAS v4u*)p;
}
#define WAIT_VM(n) asm volatile("s_waitcnt vmcnt(%0)" ::"i"(n) : "memory")
#define WAIT_LGKM0() asm volatile("s_waitcnt lgkmcnt(0)" ::: "memory")

#define STAMP(idx)
#define SSTAMP(idx)
#define SSTAMP_DECL
#define SSTAMP_FLUSH()

__device__ __forceinline__ void flag_add(lptr L, int f) {
  WAIT_LGKM0();
  if ((threadIdx.x & 63) == 0) __hip_atomic_fetch_add((LAS unsigned*)(L + L_FLAG) + f, 1u, __ATOMIC_RELAXED, __HIP_MEMORY_SCOPE_WORKGROUP);
}
__device__ __forceinline__ void flag_wait(lptr L, int f, unsigned want) {
  volatile LAS unsigned* fl = (volatile LAS unsigned*)(L + L_FLAG);
#pragma nounroll
  for (unsigned sp = 0; fl[f] < want; ++sp) {
    if (sp > SPIN_LDS || fl[FL_ABORT]) { fl[FL_ABORT] = 1; break; }
    __builtin_amdgcn_s_sleep(1);
  }
  asm volatile("" ::: "memory");
}
__device__ __forceinline__ void chain_sync(lptr L, unsigned& epoch) {
  epoch += 4;
  flag_add(L, FL_SYNC);
  flag_wait(L, FL_SYNC, epoch);
}

struct Tag { unsigned a, b; };
__device__ __forceinline__ void put(__amdgpu_buffer_rsrc_t rs, int off, unsigned lo, unsigned hi, Tag t) {
  v4u g = {lo, t.a, hi, t.b};
  __builtin_amdgcn_raw_buffer_store_b128(g, rs, off, 0, 16);
}
template <int NL, int CAN = 0>
__device__ __forceinline__ void sweep(lptr L, __amdgpu_buffer_rsrc_t rs, const int (&off)[NL], v4u (&v)[NL], Tag t) {
  volatile LAS unsigned* fl = (volatile LAS unsigned*)(L + L_FLAG);
  if (CAN > 0) {
    const int oc = __builtin_amdgcn_readlane(off[NL - 1], 63);
#pragma nounroll
    for (unsigned sp = 0;; ++sp) {
      const v4u c = __builtin_amdgcn_raw_buffer_load_b128(rs, 0, oc, 16);
      if (__all((c.y == t.a) & (c.w == t.b))) break;
      if (sp > SPIN_GLOBAL || fl[FL_ABORT]) { fl[FL_ABORT] = 1; break; }
      __builtin_amdgcn_s_sleep(CAN);
      asm volatile("" ::: "memory");
    }
  }
#pragma nounroll
  for (unsigned sp = 0;; ++sp) {
#pragma unroll
    for (int k = 0; k < NL; ++k) v[k] = __builtin_amdgcn_raw_buffer_load_b128(rs, off[k], 0, 16);
    bool ok = true;
#pragma unroll
    for (int k = 0; k < NL; ++k) ok &= (v[k].y == t.a) & (v[k].w == t.b);
    if (__all(ok)) break;
    if (sp > SPIN_GLOBAL || fl[FL_ABORT]) { fl[FL_ABORT] = 1; break; }
    asm volatile("" ::: "memory");
  }
}
__device__ __forceinline__ f32x4 gval(v4u g) { return f32x4{bf_lo(g.x), bf_hi(g.x), bf_lo(g.z), bf_hi(g.z)}; }

struct Delta { unsigned m[4]; };
__device__ __forceinline__ Delta make_delta(int lane) {
  const int j = lane & 7;
  Delta d;
#pragma unroll
  for (int r = 0; r < 4; ++r) d.m[r] = (r == (j >> 1)) ? ((j & 1) ? 0xffff0000u : 0x0000ffffu) : 0u;
  return d;
}
__device__ __forceinline__ v4u delta_op(const Delta& d, unsigned pair, int hi) {
  const unsigned dup = __builtin_amdgcn_perm(pair, pair, hi ? 0x03020302u : 0x01000100u);
  return v4u{dup & d.m[0], dup & d.m[1], dup & d.m[2], dup & d.m[3]};
}

#define DMA_TILE(o0, o1, o2, o3, base, ldsaddr, AUXS)                                                    \
  do {                                                                                                   \
    unsigned keep_;                                                                                      \
    asm volatile("s_nop 4\n\ts_mov_b32 %0, m0\n\ts_mov_b32 m0, %6\n\ts_nop 0\n\t"                        \
                 "global_load_lds_dwordx4 %1, %5" AUXS "\n\t"                                            \
                 "global_load_lds_dwordx4 %2, %5 offset:1024" AUXS "\n\t"                                \
                 "global_load_lds_dwordx4 %3, %5 offset:2048" AUXS "\n\t"                                \
                 "global_load_lds_dwordx4 %4, %5 offset:3072" AUXS "\n\t"                                \
                 "s_mov_b32 m0, %0"                                                                      \
                 : "=&s"(keep_)                                                                          \
                 : "v"(o0), "v"(o1), "v"(o2), "v"(o3), "s"(base), "s"(ldsaddr)                           \
                 : "memory");                                                                            \
  } while (0)

constexpr int last_issued(int t, int tlast) {
  int li = 7;
  for (int s = 0; s <= t; ++s) {
    const int want = s + 8 - ((8 - LOOKA) * (s + 1 < SHRINK ? s + 1 : SHRINK)) / SHRINK;
    if (want > li) li += 1;
  }
  return li < tlast ? li : tlast;
}
template <bool HASW1, int PW, bool GATED>
__device__ __forceinline__ void stream_wave(const Params& p, lptr L, int x, int i, int w, int lane) {
  constexpr int TLAST = HASW1 ? 35 : 31;
  SSTAMP_DECL;
  const int g = lane >> 4, n = lane & 15;
  const size_t key0 = (size_t)x * SK + 1024 * i + 256 * w;
  const char* Kb = (const char*)p.K + key0 * 256;
  const char* Vb = (const char*)p.V + key0 * 256;
  const int jj = 16 * x + (i - 16);
  const char* W1b = (const char*)p.w1 + (size_t)(256 * w) * (FF * 2) + jj * 64;
  const unsigned ring = (unsigned)(size_t)L + L_RING + w * 32768;
  unsigned voK[4], voV[2], voW[4];
#pragma unroll
  for (int v = 0; v < 4; ++v) voK[v] = g * 256 + ((n ^ (4 * v + g)) << 4);
#pragma unroll
  for (int v = 0; v < 2; ++v) voV[v] = g * 256 + ((n ^ (((4 * v + g) & 7) << 1)) << 4);
#pragma unroll
  for (int v = 0; v < 4; ++v) voW[v] = (16 * v + (lane >> 2)) * (FF * 2) + (((lane & 3) ^ (((lane >> 4) & 1) << 1)) << 4) - v * 1024;

#define ISSUE(t)                                                                                                     \
  do {                                                                                                               \
    const unsigned la_ = ring + ((t)&7) * 4096;                                                                      \
    if ((t) < 16) { const char* b_ = Kb + (t)*4096; DMA_TILE(voK[0], voK[1], voK[2], voK[3], b_, la_, KV_AUX); }      \
    else if ((t) < 32) { const char* b_ = Vb + ((t)-16) * 4096; DMA_TILE(voV[0], voV[1], voV[0], voV[1], b_, la_, KV_AUX); } \
    else { const char* b_ = W1b + (size_t)((t)-32) * 64 * (FF * 2); DMA_TILE(voW[0], voW[1], voW[2], voW[3], b_, la_, W1_AUX); } \
  } while (0)

  if (GATED) flag_wait(L, FL_GO, 4);
  SSTAMP(7);
#pragma unroll
  for (int t = 0; t < PW; ++t) ISSUE(t);
#pragma unroll
  for (int t = PW; t < 8; ++t) { WAIT_VM(4 * (PW - 1)); ISSUE(t); }
  SSTAMP(1);
  flag_wait(L, FL_Q, 4);
  SSTAMP(2);

  v4u qf[4];
#pragma unroll
  for (int t4 = 0; t4 < 4; ++t4) qf[t4] = lds_ld<v4u>(L + L_Q + (n & 7) * 256 + t4 * 64 + g * 16);
  lptr rb = L + L_RING + w * 32768;
  int ka[4];
#pragma unroll
  for (int t4 = 0; t4 < 4; ++t4) ka[t4] = n * 256 + (((4 * t4 + g) ^ n) << 4);

  f32x4 sc[16];
#pragma unroll
  for (int t = 0; t < 16; ++t) {
    WAIT_VM(4 * (last_issued(t - 1, TLAST) - t));
    v4u kf[4];
#pragma unroll
    for (int t4 = 0; t4 < 4; ++t4) kf[t4] = lds_ld<v4u>(rb + (t & 7) * 4096 + ka[t4]);
    WAIT_LGKM0();
    if (last_issued(t, TLAST) > last_issued(t - 1, TLAST)) ISSUE(last_issued(t, TLAST));
    f32x4 a = {0.f, 0.f, 0.f, 0.f};
#pragma unroll
    for (int t4 = 0; t4 < 4; ++t4) a = mfma32(kf[t4], qf[t4], a);
    sc[t] = a;
  }
  SSTAMP(3);
  float m = -INFINITY;
#pragma unroll
  for (int t = 0; t < 16; ++t) m = fmaxf(fmaxf(fmaxf(sc[t][0], sc[t][1]), fmaxf(sc[t][2], sc[t][3])), m);
  m = max_16_32(m);
  const float mm = m * SC_LOG2;
  float lsum = 0.f;
  v2u pb[16];
#pragma unroll
  for (int t = 0; t < 16; ++t) {
    float e[4];
#pragma unroll
    for (int r = 0; r < 4; ++r) { e[r] = __builtin_amdgcn_exp2f(fmaf(sc[t][r], SC_LOG2, -mm)); lsum += e[r]; }
    pb[t] = v2u{pack2(e[0], e[1]), pack2(e[2], e[3])};
  }
  lsum = sum_16_32(lsum);
  if (lane < 8) lds_st<v2u>(L + L_STAT + (w * 8 + lane) * 8, v2u{__float_as_uint(mm), __float_as_uint(lsum)});
  flag_add(L, FL_STAT);

  int va[8];
  {
    const int q = (lane & 15) >> 2, pp = lane & 3, row = 4 * g + q;
#pragma unroll
    for (int u = 0; u < 8; ++u) va[u] = row * 256 + (((2 * u + (pp >> 1)) ^ ((row & 7) << 1)) << 4) + (pp & 1) * 8;
  }
  f32x4 o[8];
#pragma unroll
  for (int u = 0; u < 8; ++u) o[u] = f32x4{0.f, 0.f, 0.f, 0.f};
#pragma unroll
  for (int t = 16; t < 32; ++t) {
    WAIT_VM(4 * (last_issued(t - 1, TLAST) - t));
    v2u vf[8];
#pragma unroll
    for (int u = 0; u < 8; ++u) vf[u] = lds_tr(rb + (t & 7) * 4096 + va[u]);
    WAIT_LGKM0();
    if (last_issued(t, TLAST) > last_issued(t - 1, TLAST)) ISSUE(last_issued(t, TLAST));
    if (last_issued(t, 99) == 31 && last_issued(t - 1, 99) == 30) flag_add(L, FL_LASTV);
#pragma unroll
    for (int u = 0; u < 8; ++u) o[u] = mfma16(vf[u], pb[t - 16], o[u]);
  }
  SSTAMP(4);
  flag_wait(L, FL_FAC, 1);
  const float f = lds_ld<float>(L + L_FAC + (w * 8 + (n & 7)) * 4);
  if (n < 8) {
#pragma unroll
    for (int u = 0; u < 8; ++u) lds_st<f32x4>(rb + 4 * 4096 + n * 528 + (16 * u + 4 * g) * 4, o[u] * f);
  }
  flag_add(L, FL_ODONE);
  SSTAMP(5);
  WAIT_VM(0);
  SSTAMP(6);
  SSTAMP_FLUSH();
#undef ISSUE
}

__device__ __forceinline__ f32x4 red4(lptr L, int lane) {
  f32x4 s = lds_ld<f32x4>(L + L_RED + lane * 16);
#pragma unroll
  for (int ww = 1; ww < 4; ++ww) s += lds_ld<f32x4>(L + L_RED + ww * 1024 + lane * 16);
  return s;
}

__device__ __forceinline__ f32x4 gemv_tile128(lptr L, const v4u (&W)[16], const Delta& dl, int cw, int lane) {
  const int g = lane >> 4, b = (lane >> 3) & 1;
  const v4u a0 = lds_ld<v4u>(L + L_ACT + ((b * 4 + cw) * 4 + g) * 32), a1 = lds_ld<v4u>(L + L_ACT + ((b * 4 + cw) * 4 + g) * 32 + 16);
  f32x4 acc = {0.f, 0.f, 0.f, 0.f};
#pragma unroll
  for (int u = 0; u < 8; ++u) acc = mfma32(delta_op(dl, a0[u >> 1], u & 1), W[u], acc);
#pragma unroll
  for (int u = 0; u < 8; ++u) acc = mfma32(delta_op(dl, a1[u >> 1], u & 1), W[8 + u], acc);
  return acc;
}
__device__ __forceinline__ void gemv_rows32(const v4u (&W)[16], const Delta& dl, v4u av, f32x4& acc0, f32x4& acc1) {
  acc0 = f32x4{0.f, 0.f, 0.f, 0.f}; acc1 = acc0;
#pragma unroll
  for (int u = 0; u < 8; ++u) {
    const v4u a = delta_op(dl, av[u >> 1], u & 1);
    acc0 = mfma32(a, W[u], acc0);
    acc1 = mfma32(a, W[8 + u], acc1);
  }
}
__device__ __forceinline__ void load_rows32(v4u (&W)[16], const u16* Wg, int row0, int cw, int lane) {
  const int g = lane >> 4, c = lane & 15;
#pragma unroll
  for (int cb = 0; cb < 2; ++cb)
#pragma unroll
    for (int u = 0; u < 8; ++u) W[cb * 8 + u] = ldw(Wg + (size_t)(row0 + 4 * u + g) * D + (2 * cw + cb) * 128 + 8 * c);
  __builtin_amdgcn_sched_barrier(0);
}
__device__ __forceinline__ void load_tile128(v4u (&W)[16], const u16* Wg, int ld, int row0, int col0, int cw, int lane) {
  const int g = lane >> 4, c = lane & 15;
#pragma unroll
  for (int u = 0; u < 16; ++u) W[u] = ldw(Wg + (size_t)(row0 + 64 * cw + 4 * u + g) * ld + col0 + 8 * c);
  __builtin_amdgcn_sched_barrier(0);
}
__device__ __forceinline__ void put_owner(__amdgpu_buffer_rsrc_t rs, int base, int prod, int cw, int lane, f32x4 acc0, f32x4 acc1, Tag tg) {
  const int g = lane >> 4, c = lane & 15;
  put(rs, base + (((32 * cw + c) * 128 + prod) * 4 + g) * 16, pack2(acc0[0], acc0[1]), pack2(acc0[2], acc0[3]), tg);
  put(rs, base + (((32 * cw + 16 + c) * 128 + prod) * 4 + g) * 16, pack2(acc1[0], acc1[1]), pack2(acc1[2], acc1[3]), tg);
}
template <int NP>
__device__ __forceinline__ f32x4 gather_owner(lptr L, __amdgpu_buffer_rsrc_t rs, int base, int owner, int cw, int lane, Tag tg) {
  const int t = cw * 64 + lane;
  int off[2]; v4u v[2];
  const int p1 = (t >> 2) + 64;
  off[0] = base + ((owner * 128 + (t >> 2)) * 4 + (t & 3)) * 16;
  off[1] = p1 < NP ? base + ((owner * 128 + p1) * 4 + (t & 3)) * 16 : off[0];
  sweep<2, CAN_TAIL>(L, rs, off, v, tg);
  f32x4 s = gval(v[0]);
  if (p1 < NP) s += gval(v[1]);
#pragma unroll
  for (int e = 0; e < 4; ++e) { float a = s[e]; a += dpp<ROR4>(a); a += dpp<ROR8>(a); s[e] = sum_16_32(a); }
  if (lane < 4) lds_st<f32x4>(L + L_RED + (cw * 4 + lane) * 16, s);
  WAIT_LGKM0();
  __builtin_amdgcn_s_barrier();
  f32x4 r = lds_ld<f32x4>(L + L_RED + (lane & 3) * 16);
#pragma unroll
  for (int ww = 1; ww < 4; ++ww) r += lds_ld<f32x4>(L + L_RED + (ww * 4 + (lane & 3)) * 16);
  return r;
}
__device__ __forceinline__ float silu(float v) { return v / (1.0f + __expf(-v)); }
__device__ __forceinline__ float gelu_tanh(float a) {
  const float u = 0.7978845608028654f * (a + 0.044715f * a * a * a);
  const float e = __expf(2.0f * u);
  const float th = 1.0f - 2.0f / (e + 1.0f);
  return 0.5f * a * (1.0f + th);
}

__device__ __forceinline__ void gather_q(lptr L, __amdgpu_buffer_rsrc_t rs, int x, int ct, Tag tg) {
  int off[4]; v4u v[4];
#pragma unroll
  for (int k = 0; k < 4; ++k) off[k] = WS_F2 + ((k * 2 + (x >> 2)) * 1024 + (x & 3) * 256 + ct) * 16;
  sweep<4, CAN_FRONT>(L, rs, off, v, tg);
  const f32x4 s = (gval(v[0]) + gval(v[1])) + (gval(v[2]) + gval(v[3]));
  lds_st<v2u>(L + L_Q + ct * 8, v2u{pack2(s[0], s[1]), pack2(s[2], s[3])});
  flag_add(L, FL_Q);
}
__device__ __forceinline__ void stats_exchange(lptr L, __amdgpu_buffer_rsrc_t rs, int x, int i, int lane, Tag tg) {
  flag_wait(L, FL_STAT, 4);
  const int h = lane & 7;
  float mw[4], lw[4];
#pragma unroll
  for (int w = 0; w < 4; ++w) { const v2u s = lds_ld<v2u>(L + L_STAT + (w * 8 + h) * 8); mw[w] = __uint_as_float(s.x); lw[w] = __uint_as_float(s.y); }
  const float mc = fmaxf(fmaxf(mw[0], mw[1]), fmaxf(mw[2], mw[3]));
  float lc = 0.f;
#pragma unroll
  for (int w = 0; w < 4; ++w) lc += lw[w] * __builtin_amdgcn_exp2f(mw[w] - mc);
  if (lane < 8) put(rs, WS_ST + ((x * 32 + i) * 8 + h) * 16, __float_as_uint(mc), __float_as_uint(lc), tg);
  int off[4]; v4u v[4];
#pragma unroll
  for (int k = 0; k < 4; ++k) off[k] = WS_ST + ((x * 32 + (lane >> 3) + 8 * k) * 8 + h) * 16;
  sweep<4, CAN_SLOW>(L, rs, off, v, tg);
  float M = -INFINITY;
#pragma unroll
  for (int k = 0; k < 4; ++k) M = fmaxf(M, __uint_as_float(v[k].x));
  M = fmaxf(M, dpp<ROR8>(M));
  M = max_16_32(M);
  float S = 0.f;
#pragma unroll
  for (int k = 0; k < 4; ++k) S += __uint_as_float(v[k].z) * __builtin_amdgcn_exp2f(__uint_as_float(v[k].x) - M);
  S += dpp<ROR8>(S);
  S = sum_16_32(S);
  const float inv = 1.0f / S;
  if (lane < 32) {
    const int w = lane >> 3;
    const float mws = w == 0 ? mw[0] : w == 1 ? mw[1] : w == 2 ? mw[2] : mw[3];
    lds_st<float>(L + L_FAC + lane * 4, __builtin_amdgcn_exp2f(mws - M) * inv);
  }
  flag_add(L, FL_FAC);
}
__device__ __forceinline__ void publish_partial(lptr L, __amdgpu_buffer_rsrc_t rs, int x, int i, int ct, Tag tg) {
  flag_wait(L, FL_ODONE, 4);
  const int h = ct >> 5, gi = ct & 31;
  f32x4 s = lds_ld<f32x4>(L + L_RING + 4 * 4096 + h * 528 + gi * 16);
#pragma unroll
  for (int w = 1; w < 4; ++w) s += lds_ld<f32x4>(L + L_RING + w * 32768 + 4 * 4096 + h * 528 + gi * 16);
  put(rs, WS_H1 + (((((x >> 2) * 32 + (x & 3) * 8 + h) * 4 + (gi >> 3)) * 32 + i) * 8 + (gi & 7)) * 16, pack2(s[0], s[1]), pack2(s[2], s[3]), tg);
}

__device__ __forceinline__ void chain_a(const Params& p, lptr L, __amdgpu_buffer_rsrc_t rs, int x, int i, int cw, int lane, Tag tg, unsigned long long tstart) {
  const int a = 16 * x + i, ct = cw * 64 + lane, g = lane >> 4;
  const Delta dl = make_delta(lane);
  unsigned sync = 0;
  const int cg = a >> 2, ks = a & 3, row0 = 256 * ks + 64 * cw;
  v4u W[16];
  unsigned cpair = *(const GAS unsigned*)(p.c + (lane >> 5) * D + row0 + 2 * (lane & 31));
  v2u xown = *(const GAS v2u*)(p.x + ((lane >> 1) & 1) * D + 8 * a + 4 * (lane & 1));
  v2u b2own = *(const GAS v2u*)(p.b2 + 8 * a + 4 * (lane & 1));
  __builtin_amdgcn_sched_barrier(0);
#pragma unroll
  for (int u = 0; u < 8; ++u) W[u] = ldw(p.wmod + (size_t)(row0 + 8 * u + (lane >> 3)) * NMOD + 64 * cg + 8 * (lane & 7));
  __builtin_amdgcn_sched_barrier(0);
  asm volatile("" : "+v"(cpair), "+v"(xown), "+v"(b2own), "+v"(tg.a));
  tg.a += 1u; tg.b = ~tg.a;
  if (!GATEA_Q) { asm volatile("" : "+v"(W[GOA])); flag_add(L, FL_GO); }
  {
    const int rr = 2 * (lane & 31), b = lane >> 5;
    lds_st<unsigned>(L + L_ACT + cw * 256 + (b * 64 + ((rr >> 1) & 3) * 16 + (rr >> 3) * 2) * 2, pack2(silu(bf_lo(cpair)), silu(bf_hi(cpair))));
    const int bb = (lane >> 3) & 1;
    const v4u a0 = lds_ld<v4u>(L + L_ACT + cw * 256 + (bb * 64 + g * 16) * 2), a1 = lds_ld<v4u>(L + L_ACT + cw * 256 + (bb * 64 + g * 16) * 2 + 16);
    f32x4 acc0 = {0.f, 0.f, 0.f, 0.f}, acc1 = acc0;
#pragma unroll
    for (int u = 0; u < 8; ++u) {
      const unsigned pr = u < 4 ? a0[u] : a1[u - 4];
      acc0 = mfma32(delta_op(dl, pr, 0), W[u], acc0);
      acc1 = mfma32(delta_op(dl, pr, 1), W[u], acc1);
    }
    f32x4 t = (lane & 8) ? acc1 : acc0;
#pragma unroll
    for (int e = 0; e < 4; ++e) t[e] += dpp<ROR8>(t[e]);
    lds_st<f32x4>(L + L_RED + cw * 1024 + lane * 16, t);
    chain_sync(L, sync);
    if (cw == 0 && (lane & 8) == 0) {
      const f32x4 s = red4(L, lane);
      put(rs, WS_F1 + ((ks * 2 + (g >> 1)) * 512 + 16 * cg + 2 * (lane & 7) + (g & 1)) * 16, pack2(s[0], s[1]), pack2(s[2], s[3]), tg);
    }
  }
  STAMP(1);
  if (!WO_LATE) load_rows32(W, p.wo, 32 * a, cw, lane);
  gather_q(L, rs, x, ct, tg);
  if (GATEA_Q) flag_add(L, FL_GO);
  if (WO_LATE) load_rows32(W, p.wo, 32 * a, cw, lane);
  STAMP(2);
  if (cw == 0) {
    const int v = (lane >> 2) & 3, b = (lane >> 1) & 1, half = lane & 1;
    int off[1] = {WS_F3A + (((lane >> 4) * 2 + b) * 1024 + v * 256 + 2 * a + half) * 16};
    v4u gv[1];
    sweep<1, CAN_SLOW>(L, rs, off, gv, tg);
    f32x4 s = gval(gv[0]);
#pragma unroll
    for (int e = 0; e < 4; ++e) s[e] = sum_16_32(s[e]);
    const unsigned lo = pack2(s[0], s[1]), hi = pack2(s[2], s[3]);
    if (lane < 16) {
      if (v == 1 || v == 2) put(rs, WS_F3B + ((b * 2 + v - 1) * 256 + 2 * a + half) * 16, lo, hi, tg);
      else lds_st<f32x4>(L + L_GAB + ((v ? 1 : 0) * 4 + b * 2 + half) * 16, f32x4{bf_lo(lo), bf_hi(lo), bf_lo(hi), bf_hi(hi)});
    }
    STAMP(3);
    stats_exchange(L, rs, x, i, lane, tg);
  }
  STAMP(4);
  publish_partial(L, rs, x, i, ct, tg);
  STAMP(5);
  {
    const int b = ct >> 7, sl = (ct & 127) >> 3, gi = ct & 7;
    int off[2]; v4u v[2];
#pragma unroll
    for (int k = 0; k < 2; ++k) off[k] = WS_H1 + ((((b * 32 + (a >> 2)) * 4 + (a & 3)) * 32 + sl + 16 * k) * 8 + gi) * 16;
    sweep<2, CAN_TAIL>(L, rs, off, v, tg);
    STAMP(6);
    f32x4 s = gval(v[0]) + gval(v[1]);
#pragma unroll
    for (int e = 0; e < 4; ++e) { float t = s[e]; t += dpp<ROR8>(t); s[e] = sum_16_32(t); }
    if (lane < 8) {
#pragma unroll
      for (int e = 0; e < 4; ++e) lds_st<float>(L + L_RED + ((cw * 4 + e) * 8 + lane) * 4, s[e]);
    }
    WAIT_LGKM0();
    __builtin_amdgcn_s_barrier();
    const int bb = (lane >> 3) & 1;
    lptr r0 = L + L_RED + ((2 * bb * 4 + g) * 8) * 4, r1 = r0 + 128;
    const f32x4 o0 = lds_ld<f32x4>(r0) + lds_ld<f32x4>(r1), o1 = lds_ld<f32x4>(r0 + 16) + lds_ld<f32x4>(r1 + 16);
    const v4u av = {pack2(o0[0], o0[1]), pack2(o0[2], o0[3]), pack2(o1[0], o1[1]), pack2(o1[2], o1[3])};
    f32x4 acc0, acc1;
    gemv_rows32(W, dl, av, acc0, acc1);
    put_owner(rs, WS_H2, a, cw, lane, acc0, acc1, tg);
    WAIT_LGKM0();
    __builtin_amdgcn_s_barrier();
  }
  STAMP(7);
  const f32x4 attn = gather_owner<128>(L, rs, WS_H2, a, cw, lane, tg);
  STAMP(8);
  const f32x4 ga = lds_ld<f32x4>(L + L_GAB + (lane & 3) * 16), gb = lds_ld<f32x4>(L + L_GAB + (4 + (lane & 3)) * 16);
  const f32x4 xv = {bf_lo(xown.x), bf_hi(xown.x), bf_lo(xown.y), bf_hi(xown.y)};
  f32x4 x1;
#pragma unroll
  for (int e = 0; e < 4; ++e) x1[e] = xv[e] + ga[e] * rbf(attn[e]);
  const unsigned x1lo = pack2(x1[0], x1[1]), x1hi = pack2(x1[2], x1[3]);
  if (cw == 0 && lane < 4 * NREP3) put(rs, WS_H3 + (((lane >> 2) * 128 + a) * 4 + (lane & 3)) * 16, x1lo, x1hi, tg);
  STAMP(9);
  WAIT_LGKM0();
  __builtin_amdgcn_s_barrier();
  const f32x4 mlp = gather_owner<112>(L, rs, WS_H4, a, cw, lane, tg);
  STAMP(10);
  if (cw == 0 && lane < 4) {
    const f32x4 x1r = {bf_lo(x1lo), bf_hi(x1lo), bf_lo(x1hi), bf_hi(x1hi)};
    const f32x4 bv = {bf_lo(b2own.x), bf_hi(b2own.x), bf_lo(b2own.y), bf_hi(b2own.y)};
    f32x4 r;
#pragma unroll
    for (int e = 0; e < 4; ++e) r[e] = x1r[e] + gb[e] * (mlp[e] + bv[e]);
    if (*(volatile LAS unsigned*)(L + L_FLAG + FL_ABORT * 4)) r = f32x4{NAN, NAN, NAN, NAN};
    v2u ov = v2u{pack2(r[0], r[1]), pack2(r[2], r[3])};
    if (a == 0 && lane == 0) { const unsigned long long te = __builtin_amdgcn_s_memrealtime(); if (te - tstart > (unsigned long long)(XUS * 100)) ov.x ^= 1u; }
    *(GAS v2u*)(p.out + (lane >> 1) * D + 8 * a + 4 * (lane & 1)) = ov;
    if (a == 0 && lane == 0) __hip_atomic_store((GAS unsigned*)(p.ws + WS_EPOCH), tg.a, __ATOMIC_RELAXED, __HIP_MEMORY_SCOPE_AGENT);
  }
  STAMP(11);
}

__device__ __forceinline__ void ln_rows(f32x4 (&v)[4]) {
  float s = 0.f;
#pragma unroll
  for (int k = 0; k < 4; ++k) s += (v[k][0] + v[k][1]) + (v[k][2] + v[k][3]);
  const float mean = wave_sum(s) * (1.0f / D);
  float q = 0.f;
#pragma unroll
  for (int k = 0; k < 4; ++k) { v[k] -= mean; q += (v[k][0] * v[k][0] + v[k][1] * v[k][1]) + (v[k][2] * v[k][2] + v[k][3] * v[k][3]); }
  const float r = 1.0f / sqrtf(wave_sum(q) * (1.0f / D) + LN_EPS);
#pragma unroll
  for (int k = 0; k < 4; ++k) v[k] *= r;
}
__device__ __forceinline__ void chain_b(const Params& p, lptr L, __amdgpu_buffer_rsrc_t rs, int x, int i, int cw, int lane, Tag tg) {
  const int j = 16 * x + (i - 16), ct = cw * 64 + lane, g = lane >> 4;
  const Delta dl = make_delta(lane);
  unsigned sync = 0;
  const int ks = x & 3, cgrp = (x >> 2) * 16 + (i - 16);
  v4u W[16];
  unsigned cpair = *(const GAS unsigned*)(p.c + (lane >> 5) * D + 256 * ks + 64 * cw + 2 * (lane & 31));
  unsigned b1u = *(const GAS u16*)(p.b1 + 32 * (j < 112 ? j : 0) + (lane & 31));
  v4u xa = *(const GAS v4u*)(p.x + (cw & 1) * D + 16 * lane), xb = *(const GAS v4u*)(p.x + (cw & 1) * D + 16 * lane + 8);
  v2u xs = *(const GAS v2u*)(p.x + (cw & 1) * D + 256 * ks + 4 * lane);
  __builtin_amdgcn_sched_barrier(0);
  load_tile128(W, p.wq, 4096, 256 * ks, 128 * cgrp, cw, lane);
  asm volatile("" : "+v"(cpair), "+v"(b1u), "+v"(xa), "+v"(xb), "+v"(xs), "+v"(tg.a));
  tg.a += 1u; tg.b = ~tg.a;
  if (cw < 2) {
    const int b = cw;
    float xv[16];
#pragma unroll
    for (int e = 0; e < 4; ++e) { xv[2 * e] = bf_lo(xa[e]); xv[2 * e + 1] = bf_hi(xa[e]); xv[8 + 2 * e] = bf_lo(xb[e]); xv[9 + 2 * e] = bf_hi(xb[e]); }
    float s = 0.f;
#pragma unroll
    for (int e = 0; e < 16; ++e) s += xv[e];
    const float mean = wave_sum(s) * (1.0f / D);
    float q = 0.f;
#pragma unroll
    for (int e = 0; e < 16; ++e) { const float d = xv[e] - mean; q += d * d; }
    const float rstd = 1.0f / sqrtf(wave_sum(q) * (1.0f / D) + LN_EPS);
    if (!GATEB_Q) { asm volatile("" : "+v"(W[GOB])); flag_add(L, FL_GO); }
    int off[8]; v4u v[8];
#pragma unroll
    for (int k = 0; k < 8; ++k) off[k] = WS_F1 + (((k >> 1) * 2 + b) * 512 + (k & 1) * 256 + 64 * ks + lane) * 16;
    sweep<8, CAN_FRONT>(L, rs, off, v, tg);
    const f32x4 sa = (gval(v[0]) + gval(v[2])) + (gval(v[4]) + gval(v[6])), ca = (gval(v[1]) + gval(v[3])) + (gval(v[5]) + gval(v[7]));
    const float xs4[4] = {bf_lo(xs.x), bf_hi(xs.x), bf_lo(xs.y), bf_hi(xs.y)};
#pragma unroll
    for (int e = 0; e < 4; ++e) {
      const float h = (xs4[e] - mean) * rstd * (1.0f + rbf(ca[e])) + rbf(sa[e]);
      lds_st<u16>(L + L_ACT + (((b * 4 + (lane >> 4)) * 4 + e) * 16 + (lane & 15)) * 2, (u16)pack2(h, h));
    }
  } else if (!GATEB_Q) {
    asm volatile("" : "+v"(W[GOB]));
    flag_add(L, FL_GO);
  }
  chain_sync(L, sync);
  STAMP(1);
  {
    const f32x4 acc = gemv_tile128(L, W, dl, cw, lane);
    lds_st<f32x4>(L + L_RED + cw * 1024 + lane * 16, acc);
    chain_sync(L, sync);
    if (cw == 0) {
      const f32x4 s = red4(L, lane);
      put(rs, WS_F2 + ((ks * 2 + (g >> 1)) * 1024 + 32 * cgrp + 2 * (lane & 15) + (g & 1)) * 16, pack2(s[0], s[1]), pack2(s[2], s[3]), tg);
    }
  }
  STAMP(2);
  gather_q(L, rs, x, ct, tg);
  if (GATEB_Q) flag_add(L, FL_GO);
  STAMP(3);
  load_tile128(W, p.wmod, NMOD, 256 * ks, 2048 + 128 * cgrp, cw, lane);
  {
    const int rr = 2 * (lane & 31), b = lane >> 5;
    lptr ap = L + L_ACT + (((b * 4 + cw) * 4 + (rr & 3)) * 16 + (rr >> 2)) * 2;
    const unsigned sp = pack2(silu(bf_lo(cpair)), silu(bf_hi(cpair)));
    chain_sync(L, sync);
    lds_st<u16>(ap, (u16)sp);
    lds_st<u16>(ap + 32, (u16)(sp >> 16));
    const f32x4 acc = gemv_tile128(L, W, dl, cw, lane);
    lds_st<f32x4>(L + L_RED + cw * 1024 + lane * 16, acc);
    chain_sync(L, sync);
    if (cw == 0) {
      const f32x4 s = red4(L, lane);
      put(rs, WS_F3A + ((ks * 2 + (g >> 1)) * 1024 + 32 * cgrp + 2 * (lane & 15) + (g & 1)) * 16, pack2(s[0], s[1]), pack2(s[2], s[3]), tg);
      STAMP(4);
      stats_exchange(L, rs, x, i, lane, tg);
    }
  }
  STAMP(5);
  if (j < 112) {
    int off[4]; v4u v[4];
#pragma unroll
    for (int k = 0; k < 4; ++k) off[k] = WS_F3B + (k * 256 + ct) * 16;
    sweep<4, CAN_SLOW>(L, rs, off, v, tg);
#pragma unroll
    for (int k = 0; k < 4; ++k) lds_st<v2u>(L + L_SBCB + (k * 1024 + 4 * ct) * 2, v2u{v[k].x, v[k].z});
    flag_wait(L, FL_LASTV, 4);
    load_rows32(W, p.w2, 32 * j, cw, lane);
  }
  STAMP(6);
  publish_partial(L, rs, x, i, ct, tg);
  STAMP(7);
  if (j >= 112) return;
  if (cw < 2) {
    const int b = cw;
    int off[4]; v4u v[4];
#pragma unroll
    for (int k = 0; k < 4; ++k) off[k] = WS_H3 + ((x * 128 + (k >> 1) * 64 + lane) * 4 + 2 * b + (k & 1)) * 16;
    sweep<4, CAN_TAIL>(L, rs, off, v, tg);
    STAMP(8);
    f32x4 y[4];
#pragma unroll
    for (int k = 0; k < 4; ++k) y[k] = gval(v[k]);
    ln_rows(y);
#pragma unroll
    for (int k = 0; k < 4; ++k) {
      const int col = 8 * ((k >> 1) * 64 + lane) + 4 * (k & 1);
      const v2u sb = lds_ld<v2u>(L + L_SBCB + ((b * 2 + 0) * 1024 + col) * 2), cb = lds_ld<v2u>(L + L_SBCB + ((b * 2 + 1) * 1024 + col) * 2);
      y[k][0] = y[k][0] * (1.0f + bf_lo(cb.x)) + bf_lo(sb.x);
      y[k][1] = y[k][1] * (1.0f + bf_hi(cb.x)) + bf_hi(sb.x);
      y[k][2] = y[k][2] * (1.0f + bf_lo(cb.y)) + bf_lo(sb.y);
      y[k][3] = y[k][3] * (1.0f + bf_hi(cb.y)) + bf_hi(sb.y);
    }
    ln_rows(y);
#pragma unroll
    for (int k = 0; k < 4; ++k) {
      const int gq = 2 * ((k >> 1) * 64 + lane) + (k & 1);
      const int pos = (gq >> 3) * 32 + (gq & 3) * 8 + ((gq >> 2) & 1) * 4;
      lds_st<v2u>(L + L_Y + (b * D + pos) * 2, v2u{pack2(y[k][0], y[k][1]), pack2(y[k][2], y[k][3])});
    }
  }
  WAIT_LGKM0();
  __builtin_amdgcn_s_barrier();
  STAMP(9);
  {
    f32x4 acc[2] = {{0.f, 0.f, 0.f, 0.f}, {0.f, 0.f, 0.f, 0.f}};
    const int q = (lane & 15) >> 2, pp = lane & 3;
#pragma unroll
    for (int kk = 0; kk < 8; ++kk) {
      const v4u ya = lds_ld<v4u>(L + L_Y + ((lane & 1) * D + 256 * cw + 32 * kk + 8 * g) * 2);
#pragma unroll
      for (int ch = 0; ch < 2; ++ch) {
        v2u w[2];
#pragma unroll
        for (int hh = 0; hh < 2; ++hh) {
          const int row = (kk & 1) * 32 + 16 * hh + 4 * g + q;
          const int pc = (2 * ch + (pp >> 1)) ^ (((row >> 2) & 1) << 1);
          w[hh] = lds_tr(L + L_RING + cw * 32768 + (kk >> 1) * 4096 + row * 64 + pc * 16 + (pp & 1) * 8);
        }
        acc[ch] = mfma32(ya, v4u{w[0].x, w[0].y, w[1].x, w[1].y}, acc[ch]);
      }
    }
    if (lane < 16) {
#pragma unroll
      for (int ch = 0; ch < 2; ++ch)
#pragma unroll
        for (int b = 0; b < 2; ++b) lds_st<float>(L + L_RED + (cw * 64 + b * 32 + ch * 16 + lane) * 4, acc[ch][b]);
    }
    WAIT_LGKM0();
    __builtin_amdgcn_s_barrier();
    float a = bf_lo(b1u);
#pragma unroll
    for (int ww = 0; ww < 4; ++ww) a += lds_ld<float>(L + L_RED + (ww * 64 + lane) * 4);
    const float hv = gelu_tanh(rbf(a));
    const int k = lane & 31;
    lds_st<u16>(L + L_HID + cw * 128 + ((lane >> 5) * 32 + (k & 3) * 8 + (k >> 2)) * 2, (u16)pack2(hv, hv));
    const v4u av = lds_ld<v4u>(L + L_HID + cw * 128 + (((lane >> 3) & 1) * 32 + g * 8) * 2);
    f32x4 acc0, acc1;
    gemv_rows32(W, dl, av, acc0, acc1);
    put_owner(rs, WS_H4, j, cw, lane, acc0, acc1, tg);
  }
  STAMP(10);
}

__global__ void __launch_bounds__(512, 2) fused_decode_block(Params p) {
  extern __shared__ __attribute__((aligned(16))) unsigned char smem[];
  lptr L = (lptr)smem;
  const int tid = threadIdx.x, lane = tid & 63, wave = __builtin_amdgcn_readfirstlane(tid >> 6);
  const int x = blockIdx.x & 7, i = blockIdx.x >> 3;
  const unsigned long long tstart = __builtin_amdgcn_s_memrealtime();
  STAMP(0);
  if (tid < 16) *(LAS unsigned*)(L + L_FLAG + tid * 4) = 0u;
  WAIT_LGKM0();
  __builtin_amdgcn_s_barrier();
  if (wave < 4) {
    if (i < 16) stream_wave<false, PREW, GATE != 0 || GATEA_Q != 0>(p, L, x, i, wave, lane);
    else if (16 * x + (i - 16) < 112) stream_wave<true, PREWB, GATE != 0 || GATEB_Q != 0>(p, L, x, i, wave, lane);
    else stream_wave<false, PREWB, GATE != 0 || GATEB_Q != 0>(p, L, x, i, wave, lane);
    return;
  }
  __builtin_amdgcn_s_setprio(2);
  const __amdgpu_buffer_rsrc_t rs = __builtin_amdgcn_make_buffer_rsrc((void*)p.ws, 0, WS_END, 0x00020000);
  const Tag tg = {__hip_atomic_load((GAS unsigned*)(p.ws + WS_EPOCH), __ATOMIC_RELAXED, __HIP_MEMORY_SCOPE_AGENT), 0u};
  if (i < 16) chain_a(p, L, rs, x, i, wave - 4, lane, tg, tstart);
  else chain_b(p, L, rs, x, i, wave - 4, lane, tg);
}

}

extern "C" void kernel_launch(void* const* d_in, const int* in_sizes, int n_in, void* d_out, int out_size, void* d_ws, size_t ws_size,
                              hipStream_t stream) {
  Params p;
  p.x = (const u16*)d_in[0];
  p.K = (const u16*)d_in[1];
  p.V = (const u16*)d_in[2];
  p.wq = (const u16*)d_in[3];
  p.wo = (const u16*)d_in[4];
  p.w1 = (const u16*)d_in[6];
  p.b1 = (const u16*)d_in[7];
  p.w2 = (const u16*)d_in[8];
  p.b2 = (const u16*)d_in[9];
  p.c = (const u16*)d_in[10];
  p.wmod = (const u16*)d_in[11];
  p.out = (u16*)d_out;
  p.ws = (unsigned char*)d_ws;
  (void)hipFuncSetAttribute((const void*)fused_decode_block, hipFuncAttributeMaxDynamicSharedMemorySize, LDS_BYTES);
  hipLaunchKernelGGL(fused_decode_block, dim3(256), dim3(512), LDS_BYTES, stream, p);
}
```

```cpp
#define GATE 0
#define WBL2 1
#include <hip/hip_runtime.h>
#include <hip/hip_bf16.h>
#include <stdint.h>

#define GAS __attribute__((address_space(1)))
#define LAS __attribute__((address_space(3)))
typedef unsigned v4u __attribute__((ext_vector_type(4)));
typedef unsigned v2u __attribute__((ext_vector_type(2)));
typedef float f32x4 __attribute__((ext_vector_type(4)));
typedef __bf16 bf16x8 __attribute__((ext_vector_type(8)));
typedef __bf16 bf16x2 __attribute__((ext_vector_type(2)));
typedef short s16x4 __attribute__((ext_vector_type(4)));
typedef unsigned short u16;
typedef LAS unsigned char* lptr;

#define STAMPS 0
#define PREW 2
#define LOOKA 8
#define SHRINK 16
#define CAN_FRONT 2
#define CAN_TAIL 1
#define CAN_SLOW 16
#define GOA 3
#define GOB 7
#define W1_AUX " nt"
#define PREWB PREW
#define GATEB_Q 0
#define GATEA_Q 0
#define WO_LATE 0
#define KV_AUX " nt"

namespace {

constexpr int D = 1024, SK = 32768, FF = 3584, NMOD = 6144;
constexpr float SC_LOG2 = 0.08838834764831845f * 1.4426950408889634f;
constexpr float LN_EPS = 1e-5f;

constexpr int WS_EPOCH = 0;
constexpr int WS_F1  = 4096;
constexpr int WS_F2  = WS_F1 + 4096 * 16;
constexpr int WS_F3A = WS_F2 + 8192 * 16;
constexpr int WS_F3B = WS_F3A + 8192 * 16;
constexpr int WS_ST  = WS_F3B + 1024 * 16;
constexpr int WS_H1  = WS_ST + 2048 * 16;
constexpr int WS_H2  = WS_H1 + 65536 * 16;
constexpr int NREP3  = 8;
constexpr int WS_H3  = WS_H2 + 65536 * 16;
constexpr int WS_H4  = WS_H3 + NREP3 * 512 * 16;
constexpr int WS_END = WS_H4 + 65536 * 16;
constexpr int WS_STAMP = 8 << 20;

constexpr int L_RING = 0;
constexpr int L_MISC = 131072;
constexpr int L_ACT  = L_MISC;
constexpr int L_Q    = L_ACT + 1024;
constexpr int L_RED  = L_Q + 2048;
constexpr int L_SBCB = L_RED + 4096;
constexpr int L_Y    = L_SBCB + 8192;
constexpr int L_STAT = L_Y + 4096;
constexpr int L_FAC  = L_STAT + 256;
constexpr int L_GAB  = L_FAC + 128;
constexpr int L_HID  = L_GAB + 128;
constexpr int L_FLAG = L_HID + 512;
constexpr int LDS_BYTES = 163840;
static_assert(L_FLAG + 64 <= LDS_BYTES, "LDS map");
enum { FL_SYNC = 0, FL_Q = 1, FL_STAT = 2, FL_FAC = 3, FL_ODONE = 4, FL_LASTV = 5, FL_ABORT = 6, FL_GO = 7 };

constexpr unsigned SPIN_GLOBAL = 1u << 17;
constexpr unsigned SPIN_LDS = 1u << 22;

struct Params {
  const u16 *x, *K, *V, *wq, *wo, *w1, *b1, *w2, *b2, *c, *wmod;
  u16* out;
  unsigned char* ws;
};

__device__ __forceinline__ float bf_lo(unsigned u) { return __builtin_bit_cast(float, u << 16); }
__device__ __forceinline__ float bf_hi(unsigned u) { return __builtin_bit_cast(float, u & 0xffff0000u); }
__device__ __forceinline__ unsigned pack2(float lo, float hi) { bf16x2 r = {(__bf16)lo, (__bf16)hi}; return __builtin_bit_cast(unsigned, r); }
__device__ __forceinline__ float rbf(float v) { return bf_lo(pack2(v, v) & 0xffffu); }
template <int CTRL> __device__ __forceinline__ float dpp(float v) {
  return __builtin_bit_cast(float, __builtin_amdgcn_mov_dpp(__builtin_bit_cast(int, v), CTRL, 0xf, 0xf, true));
}
constexpr int XOR1 = 0xB1, XOR2 = 0x4E, ROR4 = 0x124, ROR8 = 0x128;
__device__ __forceinline__ float sum_16_32(float v) {
  auto s = __builtin_amdgcn_permlane16_swap(__float_as_uint(v), __float_as_uint(v), false, false);
  v = __uint_as_float(s[0]) + __uint_as_float(s[1]);
  auto t = __builtin_amdgcn_permlane32_swap(__float_as_uint(v), __float_as_uint(v), false, false);
  return __uint_as_float(t[0]) + __uint_as_float(t[1]);
}
__device__ __forceinline__ float max_16_32(float v) {
  auto s = __builtin_amdgcn_permlane16_swap(__float_as_uint(v), __float_as_uint(v), false, false);
  v = fmaxf(__uint_as_float(s[0]), __uint_as_float(s[1]));
  auto t = __builtin_amdgcn_permlane32_swap(__float_as_uint(v), __float_as_uint(v), false, false);
  return fmaxf(__uint_as_float(t[0]), __uint_as_float(t[1]));
}
__device__ __forceinline__ float wave_sum(float v) {
  v += dpp<XOR1>(v); v += dpp<XOR2>(v); v += dpp<ROR4>(v); v += dpp<ROR8>(v);
  return sum_16_32(v);
}
__device__ __forceinline__ f32x4 mfma32(v4u a, v4u b, f32x4 c) {
  return __builtin_amdgcn_mfma_f32_16x16x32_bf16(__builtin_bit_cast(bf16x8, a), __builtin_bit_cast(bf16x8, b), c, 0, 0, 0);
}
__device__ __forceinline__ f32x4 mfma16(v2u a, v2u b, f32x4 c) {
  return __builtin_amdgcn_mfma_f32_16x16x16bf16_1k(__builtin_bit_cast(s16x4, a), __builtin_bit_cast(s16x4, b), c, 0, 0, 0);
}
__device__ __forceinline__ v2u lds_tr(lptr p) {
  return __builtin_bit_cast(v2u, __builtin_amdgcn_ds_read_tr16_b64_v4i16((LAS s16x4*)p));
}
template <class T> __device__ __forceinline__ T lds_ld(lptr p) { return *(LAS T*)p; }
template <class T> __device__ __forceinline__ void lds_st(lptr p, T v) { *(LAS T*)p = v; }
#define W_NT 1
__device__ __forceinline__ v4u ldw(const u16* p) {
  if (W_NT) return __builtin_nontemporal_load((const GAS v4u*)p);
  return *(const GAS v4u*)p;
}
#define WAIT_VM(n) asm volatile("s_waitcnt vmcnt(%0)" ::"i"(n) : "memory")
#define WAIT_LGKM0() asm volatile("s_waitcnt lgkmcnt(0)" ::: "memory")

#define STAMP(idx)
#define SSTAMP(idx)
#define SSTAMP_DECL
#define SSTAMP_FLUSH()

__device__ __forceinline__ void flag_add(lptr L, int f) {
  WAIT_LGKM0();
  if ((threadIdx.x & 63) == 0) __hip_atomic_fetch_add((LAS unsigned*)(L + L_FLAG) + f, 1u, __ATOMIC_RELAXED, __HIP_MEMORY_SCOPE_WORKGROUP);
}
__device__ __forceinline__ void flag_wait(lptr L, int f, unsigned want) {
  volatile LAS unsigned* fl = (volatile LAS unsigned*)(L + L_FLAG);
#pragma nounroll
  for (unsigned sp = 0; fl[f] < want; ++sp) {
    if (sp > SPIN_LDS || fl[FL_ABORT]) { fl[FL_ABORT] = 1; break; }
    __builtin_amdgcn_s_sleep(1);
  }
  asm volatile("" ::: "memory");
}
__device__ __forceinline__ void chain_sync(lptr L, unsigned& epoch) {
  epoch += 4;
  flag_add(L, FL_SYNC);
  flag_wait(L, FL_SYNC, epoch);
}

struct Tag { unsigned a, b; };
__device__ __forceinline__ void put(__amdgpu_buffer_rsrc_t rs, int off, unsigned lo, unsigned hi, Tag t) {
  v4u g = {lo, t.a, hi, t.b};
  __builtin_amdgcn_raw_buffer_store_b128(g, rs, off, 0, 16);
}
template <int NL, int CAN = 0>
__device__ __forceinline__ void sweep(lptr L, __amdgpu_buffer_rsrc_t rs, const int (&off)[NL], v4u (&v)[NL], Tag t) {
  volatile LAS unsigned* fl = (volatile LAS unsigned*)(L + L_FLAG);
  if (CAN > 0) {
    const int oc = __builtin_amdgcn_readlane(off[NL - 1], 63);
#pragma nounroll
    for (unsigned sp = 0;; ++sp) {
      const v4u c = __builtin_amdgcn_raw_buffer_load_b128(rs, 0, oc, 16);
      if (__all((c.y == t.a) & (c.w == t.b))) break;
      if (sp > SPIN_GLOBAL || fl[FL_ABORT]) { fl[FL_ABORT] = 1; break; }
      __builtin_amdgcn_s_sleep(CAN);
      asm volatile("" ::: "memory");
    }
  }
#pragma nounroll
  for (unsigned sp = 0;; ++sp) {
#pragma unroll
    for (int k = 0; k < NL; ++k) v[k] = __builtin_amdgcn_raw_buffer_load_b128(rs, off[k], 0, 16);
    bool ok = true;
#pragma unroll
    for (int k = 0; k < NL; ++k) ok &= (v[k].y == t.a) & (v[k].w == t.b);
    if (__all(ok)) break;
    if (sp > SPIN_GLOBAL || fl[FL_ABORT]) { fl[FL_ABORT] = 1; break; }
    asm volatile("" ::: "memory");
  }
}
__device__ __forceinline__ f32x4 gval(v4u g) { return f32x4{bf_lo(g.x), bf_hi(g.x), bf_lo(g.z), bf_hi(g.z)}; }

struct Delta { unsigned m[4]; };
__device__ __forceinline__ Delta make_delta(int lane) {
  const int j = lane & 7;
  Delta d;
#pragma unroll
  for (int r = 0; r < 4; ++r) d.m[r] = (r == (j >> 1)) ? ((j & 1) ? 0xffff0000u : 0x0000ffffu) : 0u;
  return d;
}
__device__ __forceinline__ v4u delta_op(const Delta& d, unsigned pair, int hi) {
  const unsigned dup = __builtin_amdgcn_perm(pair, pair, hi ? 0x03020302u : 0x01000100u);
  return v4u{dup & d.m[0], dup & d.m[1], dup & d.m[2], dup & d.m[3]};
}

#define DMA_TILE(o0, o1, o2, o3, base, ldsaddr, AUXS)                                                    \
  do {                                                                                                   \
    unsigned keep_;                                                                                      \
    asm volatile("s_nop 4\n\ts_mov_b32 %0, m0\n\ts_mov_b32 m0, %6\n\ts_nop 0\n\t"                        \
                 "global_load_lds_dwordx4 %1, %5" AUXS "\n\t"                                            \
                 "global_load_lds_dwordx4 %2, %5 offset:1024" AUXS "\n\t"                                \
                 "global_load_lds_dwordx4 %3, %5 offset:2048" AUXS "\n\t"                                \
                 "global_load_lds_dwordx4 %4, %5 offset:3072" AUXS "\n\t"                                \
                 "s_mov_b32 m0, %0"                                                                      \
                 : "=&s"(keep_)                                                                          \
                 : "v"(o0), "v"(o1), "v"(o2), "v"(o3), "s"(base), "s"(ldsaddr)                           \
                 : "memory");                                                                            \
  } while (0)

constexpr int last_issued(int t, int tlast) {
  int li = 7;
  for (int s = 0; s <= t; ++s) {
    const int want = s + 8 - ((8 - LOOKA) * (s + 1 < SHRINK ? s + 1 : SHRINK)) / SHRINK;
    if (want > li) li += 1;
  }
  return li < tlast ? li : tlast;
}
template <bool HASW1, int PW, bool GATED>
__device__ __forceinline__ void stream_wave(const Params& p, lptr L, int x, int i, int w, int lane) {
  constexpr int TLAST = HASW1 ? 35 : 31;
  SSTAMP_DECL;
  const int g = lane >> 4, n = lane & 15;
  const size_t key0 = (size_t)x * SK + 1024 * i + 256 * w;
  const char* Kb = (const char*)p.K + key0 * 256;
  const char* Vb = (const char*)p.V + key0 * 256;
  const int jj = 16 * x + (i - 16);
  const char* W1b = (const char*)p.w1 + (size_t)(256 * w) * (FF * 2) + jj * 64;
  const unsigned ring = (unsigned)(size_t)L + L_RING + w * 32768;
  unsigned voK[4], voV[2], voW[4];
#pragma unroll
  for (int v = 0; v < 4; ++v) voK[v] = g * 256 + ((n ^ (4 * v + g)) << 4);
#pragma unroll
  for (int v = 0; v < 2; ++v) voV[v] = g * 256 + ((n ^ (((4 * v + g) & 7) << 1)) << 4);
#pragma unroll
  for (int v = 0; v < 4; ++v) voW[v] = (16 * v + (lane >> 2)) * (FF * 2) + (((lane & 3) ^ (((lane >> 4) & 1) << 1)) << 4) - v * 1024;

#define ISSUE(t)                                                                                                     \
  do {                                                                                                               \
    const unsigned la_ = ring + ((t)&7) * 4096;                                                                      \
    if ((t) < 16) { const char* b_ = Kb + (t)*4096; DMA_TILE(voK[0], voK[1], voK[2], voK[3], b_, la_, KV_AUX); }      \
    else if ((t) < 32) { const char* b_ = Vb + ((t)-16) * 4096; DMA_TILE(voV[0], voV[1], voV[0], voV[1], b_, la_, KV_AUX); } \
    else { const char* b_ = W1b + (size_t)((t)-32) * 64 * (FF * 2); DMA_TILE(voW[0], voW[1], voW[2], voW[3], b_, la_, W1_AUX); } \
  } while (0)

  if (GATED) flag_wait(L, FL_GO, 4);
  SSTAMP(7);
#pragma unroll
  for (int t = 0; t < PW; ++t) ISSUE(t);
#pragma unroll
  for (int t = PW; t < 8; ++t) { WAIT_VM(4 * (PW - 1)); ISSUE(t); }
  SSTAMP(1);
  flag_wait(L, FL_Q, 4);
  SSTAMP(2);

  v4u qf[4];
#pragma unroll
  for (int t4 = 0; t4 < 4; ++t4) qf[t4] = lds_ld<v4u>(L + L_Q + (n & 7) * 256 + t4 * 64 + g * 16);
  lptr rb = L + L_RING + w * 32768;
  int ka[4];
#pragma unroll
  for (int t4 = 0; t4 < 4; ++t4) ka[t4] = n * 256 + (((4 * t4 + g) ^ n) << 4);

  f32x4 sc[16];
#pragma unroll
  for (int t = 0; t < 16; ++t) {
    WAIT_VM(4 * (last_issued(t - 1, TLAST) - t));
    v4u kf[4];
#pragma unroll
    for (int t4 = 0; t4 < 4; ++t4) kf[t4] = lds_ld<v4u>(rb + (t & 7) * 4096 + ka[t4]);
    WAIT_LGKM0();
    if (last_issued(t, TLAST) > last_issued(t - 1, TLAST)) ISSUE(last_issued(t, TLAST));
    f32x4 a = {0.f, 0.f, 0.f, 0.f};
#pragma unroll
    for (int t4 = 0; t4 < 4; ++t4) a = mfma32(kf[t4], qf[t4], a);
    sc[t] = a;
  }
  SSTAMP(3);
  float m = -INFINITY;
#pragma unroll
  for (int t = 0; t < 16; ++t) m = fmaxf(fmaxf(fmaxf(sc[t][0], sc[t][1]), fmaxf(sc[t][2], sc[t][3])), m);
  m = max_16_32(m);
  const float mm = m * SC_LOG2;
  float lsum = 0.f;
  v2u pb[16];
#pragma unroll
  for (int t = 0; t < 16; ++t) {
    float e[4];
#pragma unroll
    for (int r = 0; r < 4; ++r) { e[r] = __builtin_amdgcn_exp2f(fmaf(sc[t][r], SC_LOG2, -mm)); lsum += e[r]; }
    pb[t] = v2u{pack2(e[0], e[1]), pack2(e[2], e[3])};
  }
  lsum = sum_16_32(lsum);
  if (lane < 8) lds_st<v2u>(L + L_STAT + (w * 8 + lane) * 8, v2u{__float_as_uint(mm), __float_as_uint(lsum)});
  flag_add(L, FL_STAT);

  int va[8];
  {
    const int q = (lane & 15) >> 2, pp = lane & 3, row = 4 * g + q;
#pragma unroll
    for (int u = 0; u < 8; ++u) va[u] = row * 256 + (((2 * u + (pp >> 1)) ^ ((row & 7) << 1)) << 4) + (pp & 1) * 8;
  }
  f32x4 o[8];
#pragma unroll
  for (int u = 0; u < 8; ++u) o[u] = f32x4{0.f, 0.f, 0.f, 0.f};
#pragma unroll
  for (int t = 16; t < 32; ++t) {
    WAIT_VM(4 * (last_issued(t - 1, TLAST) - t));
    v2u vf[8];
#pragma unroll
    for (int u = 0; u < 8; ++u) vf[u] = lds_tr(rb + (t & 7) * 4096 + va[u]);
    WAIT_LGKM0();
    if (last_issued(t, TLAST) > last_issued(t - 1, TLAST)) ISSUE(last_issued(t, TLAST));
    if (last_issued(t, 99) == 31 && last_issued(t - 1, 99) == 30) flag_add(L, FL_LASTV);
#pragma unroll
    for (int u = 0; u < 8; ++u) o[u] = mfma16(vf[u], pb[t - 16], o[u]);
  }
  SSTAMP(4);
  flag_wait(L, FL_FAC, 1);
  const float f = lds_ld<float>(L + L_FAC + (w * 8 + (n & 7)) * 4);
  if (n < 8) {
#pragma unroll
    for (int u = 0; u < 8; ++u) lds_st<f32x4>(rb + 4 * 4096 + n * 528 + (16 * u + 4 * g) * 4, o[u] * f);
  }
  flag_add(L, FL_ODONE);
  SSTAMP(5);
  WAIT_VM(0);
  SSTAMP(6);
  SSTAMP_FLUSH();
#undef ISSUE
}

__device__ __forceinline__ f32x4 red4(lptr L, int lane) {
  f32x4 s = lds_ld<f32x4>(L + L_RED + lane * 16);
#pragma unroll
  for (int ww = 1; ww < 4; ++ww) s += lds_ld<f32x4>(L + L_RED + ww * 1024 + lane * 16);
  return s;
}

__device__ __forceinline__ f32x4 gemv_tile128(lptr L, const v4u (&W)[16], const Delta& dl, int cw, int lane) {
  const int g = lane >> 4, b = (lane >> 3) & 1;
  const v4u a0 = lds_ld<v4u>(L + L_ACT + ((b * 4 + cw) * 4 + g) * 32), a1 = lds_ld<v4u>(L + L_ACT + ((b * 4 + cw) * 4 + g) * 32 + 16);
  f32x4 acc = {0.f, 0.f, 0.f, 0.f};
#pragma unroll
  for (int u = 0; u < 8; ++u) acc = mfma32(delta_op(dl, a0[u >> 1], u & 1), W[u], acc);
#pragma unroll
  for (int u = 0; u < 8; ++u) acc = mfma32(delta_op(dl, a1[u >> 1], u & 1), W[8 + u], acc);
  return acc;
}
__device__ __forceinline__ void gemv_rows32(const v4u (&W)[16], const Delta& dl, v4u av, f32x4& acc0, f32x4& acc1) {
  acc0 = f32x4{0.f, 0.f, 0.f, 0.f}; acc1 = acc0;
#pragma unroll
  for (int u = 0; u < 8; ++u) {
    const v4u a = delta_op(dl, av[u >> 1], u & 1);
    acc0 = mfma32(a, W[u], acc0);
    acc1 = mfma32(a, W[8 + u], acc1);
  }
}
__device__ __forceinline__ void load_rows32(v4u (&W)[16], const u16* Wg, int row0, int cw, int lane) {
  const int g = lane >> 4, c = lane & 15;
#pragma unroll
  for (int cb = 0; cb < 2; ++cb)
#pragma unroll
    for (int u = 0; u < 8; ++u) W[cb * 8 + u] = ldw(Wg + (size_t)(row0 + 4 * u + g) * D + (2 * cw + cb) * 128 + 8 * c);
  __builtin_amdgcn_sched_barrier(0);
}
__device__ __forceinline__ void load_tile128(v4u (&W)[16], const u16* Wg, int ld, int row0, int col0, int cw, int lane) {
  const int g = lane >> 4, c = lane & 15;
#pragma unroll
  for (int u = 0; u < 16; ++u) W[u] = ldw(Wg + (size_t)(row0 + 64 * cw + 4 * u + g) * ld + col0 + 8 * c);
  __builtin_amdgcn_sched_barrier(0);
}
__device__ __forceinline__ void put_owner(__amdgpu_buffer_rsrc_t rs, int base, int prod, int cw, int lane, f32x4 acc0, f32x4 acc1, Tag tg) {
  const int g = lane >> 4, c = lane & 15;
  put(rs, base + (((32 * cw + c) * 128 + prod) * 4 + g) * 16, pack2(acc0[0], acc0[1]), pack2(acc0[2], acc0[3]), tg);
  put(rs, base + (((32 * cw + 16 + c) * 128 + prod) * 4 + g) * 16, pack2(acc1[0], acc1[1]), pack2(acc1[2], acc1[3]), tg);
}
template <int NP>
__device__ __forceinline__ f32x4 gather_owner(lptr L, __amdgpu_buffer_rsrc_t rs, int base, int owner, int cw, int lane, Tag tg) {
  const int t = cw * 64 + lane;
  int off[2]; v4u v[2];
  const int p1 = (t >> 2) + 64;
  off[0] = base + ((owner * 128 + (t >> 2)) * 4 + (t & 3)) * 16;
  off[1] = p1 < NP ? base + ((owner * 128 + p1) * 4 + (t & 3)) * 16 : off[0];
  sweep<2, CAN_TAIL>(L, rs, off, v, tg);
  f32x4 s = gval(v[0]);
  if (p1 < NP) s += gval(v[1]);
#pragma unroll
  for (int e = 0; e < 4; ++e) { float a = s[e]; a += dpp<ROR4>(a); a += dpp<ROR8>(a); s[e] = sum_16_32(a); }
  if (lane < 4) lds_st<f32x4>(L + L_RED + (cw * 4 + lane) * 16, s);
  WAIT_LGKM0();
  __builtin_amdgcn_s_barrier();
  f32x4 r = lds_ld<f32x4>(L + L_RED + (lane & 3) * 16);
#pragma unroll
  for (int ww = 1; ww < 4; ++ww) r += lds_ld<f32x4>(L + L_RED + (ww * 4 + (lane & 3)) * 16);
  return r;
}
__device__ __forceinline__ float silu(float v) { return v / (1.0f + __expf(-v)); }
__device__ __forceinline__ float gelu_tanh(float a) {
  const float u = 0.7978845608028654f * (a + 0.044715f * a * a * a);
  const float e = __expf(2.0f * u);
  const float th = 1.0f - 2.0f / (e + 1.0f);
  return 0.5f * a * (1.0f + th);
}

__device__ __forceinline__ void gather_q(lptr L, __amdgpu_buffer_rsrc_t rs, int x, int ct, Tag tg) {
  int off[4]; v4u v[4];
#pragma unroll
  for (int k = 0; k < 4; ++k) off[k] = WS_F2 + ((k * 2 + (x >> 2)) * 1024 + (x & 3) * 256 + ct) * 16;
  sweep<4, CAN_FRONT>(L, rs, off, v, tg);
  const f32x4 s = (gval(v[0]) + gval(v[1])) + (gval(v[2]) + gval(v[3]));
  lds_st<v2u>(L + L_Q + ct * 8, v2u{pack2(s[0], s[1]), pack2(s[2], s[3])});
  flag_add(L, FL_Q);
}
__device__ __forceinline__ void stats_exchange(lptr L, __amdgpu_buffer_rsrc_t rs, int x, int i, int lane, Tag tg) {
  flag_wait(L, FL_STAT, 4);
  const int h = lane & 7;
  float mw[4], lw[4];
#pragma unroll
  for (int w = 0; w < 4; ++w) { const v2u s = lds_ld<v2u>(L + L_STAT + (w * 8 + h) * 8); mw[w] = __uint_as_float(s.x); lw[w] = __uint_as_float(s.y); }
  const float mc = fmaxf(fmaxf(mw[0], mw[1]), fmaxf(mw[2], mw[3]));
  float lc = 0.f;
#pragma unroll
  for (int w = 0; w < 4; ++w) lc += lw[w] * __builtin_amdgcn_exp2f(mw[w] - mc);
  if (lane < 8) put(rs, WS_ST + ((x * 32 + i) * 8 + h) * 16, __float_as_uint(mc), __float_as_uint(lc), tg);
  int off[4]; v4u v[4];
#pragma unroll
  for (int k = 0; k < 4; ++k) off[k] = WS_ST + ((x * 32 + (lane >> 3) + 8 * k) * 8 + h) * 16;
  sweep<4, CAN_SLOW>(L, rs, off, v, tg);
  float M = -INFINITY;
#pragma unroll
  for (int k = 0; k < 4; ++k) M = fmaxf(M, __uint_as_float(v[k].x));
  M = fmaxf(M, dpp<ROR8>(M));
  M = max_16_32(M);
  float S = 0.f;
#pragma unroll
  for (int k = 0; k < 4; ++k) S += __uint_as_float(v[k].z) * __builtin_amdgcn_exp2f(__uint_as_float(v[k].x) - M);
  S += dpp<ROR8>(S);
  S = sum_16_32(S);
  const float inv = 1.0f / S;
  if (lane < 32) {
    const int w = lane >> 3;
    const float mws = w == 0 ? mw[0] : w == 1 ? mw[1] : w == 2 ? mw[2] : mw[3];
    lds_st<float>(L + L_FAC + lane * 4, __builtin_amdgcn_exp2f(mws - M) * inv);
  }
  flag_add(L, FL_FAC);
}
__device__ __forceinline__ void publish_partial(lptr L, __amdgpu_buffer_rsrc_t rs, int x, int i, int ct, Tag tg) {
  flag_wait(L, FL_ODONE, 4);
  const int h = ct >> 5, gi = ct & 31;
  f32x4 s = lds_ld<f32x4>(L + L_RING + 4 * 4096 + h * 528 + gi * 16);
#pragma unroll
  for (int w = 1; w < 4; ++w) s += lds_ld<f32x4>(L + L_RING + w * 32768 + 4 * 4096 + h * 528 + gi * 16);
  put(rs, WS_H1 + (((((x >> 2) * 32 + (x & 3) * 8 + h) * 4 + (gi >> 3)) * 32 + i) * 8 + (gi & 7)) * 16, pack2(s[0], s[1]), pack2(s[2], s[3]), tg);
}

__device__ __forceinline__ void chain_a(const Params& p, lptr L, __amdgpu_buffer_rsrc_t rs, int x, int i, int cw, int lane, Tag tg) {
  const int a = 16 * x + i, ct = cw * 64 + lane, g = lane >> 4;
  const Delta dl = make_delta(lane);
  unsigned sync = 0;
  const int cg = a >> 2, ks = a & 3, row0 = 256 * ks + 64 * cw;
  v4u W[16];
  unsigned cpair = *(const GAS unsigned*)(p.c + (lane >> 5) * D + row0 + 2 * (lane & 31));
  v2u xown = *(const GAS v2u*)(p.x + ((lane >> 1) & 1) * D + 8 * a + 4 * (lane & 1));
  v2u b2own = *(const GAS v2u*)(p.b2 + 8 * a + 4 * (lane & 1));
  __builtin_amdgcn_sched_barrier(0);
#pragma unroll
  for (int u = 0; u < 8; ++u) W[u] = ldw(p.wmod + (size_t)(row0 + 8 * u + (lane >> 3)) * NMOD + 64 * cg + 8 * (lane & 7));
  __builtin_amdgcn_sched_barrier(0);
  asm volatile("" : "+v"(cpair), "+v"(xown), "+v"(b2own), "+v"(tg.a));
  tg.a += 1u; tg.b = ~tg.a;
  if (!GATEA_Q) { asm volatile("" : "+v"(W[GOA])); flag_add(L, FL_GO); }
  {
    const int rr = 2 * (lane & 31), b = lane >> 5;
    lds_st<unsigned>(L + L_ACT + cw * 256 + (b * 64 + ((rr >> 1) & 3) * 16 + (rr >> 3) * 2) * 2, pack2(silu(bf_lo(cpair)), silu(bf_hi(cpair))));
    const int bb = (lane >> 3) & 1;
    const v4u a0 = lds_ld<v4u>(L + L_ACT + cw * 256 + (bb * 64 + g * 16) * 2), a1 = lds_ld<v4u>(L + L_ACT + cw * 256 + (bb * 64 + g * 16) * 2 + 16);
    f32x4 acc0 = {0.f, 0.f, 0.f, 0.f}, acc1 = acc0;
#pragma unroll
    for (int u = 0; u < 8; ++u) {
      const unsigned pr = u < 4 ? a0[u] : a1[u - 4];
      acc0 = mfma32(delta_op(dl, pr, 0), W[u], acc0);
      acc1 = mfma32(delta_op(dl, pr, 1), W[u], acc1);
    }
    f32x4 t = (lane & 8) ? acc1 : acc0;
#pragma unroll
    for (int e = 0; e < 4; ++e) t[e] += dpp<ROR8>(t[e]);
    lds_st<f32x4>(L + L_RED + cw * 1024 + lane * 16, t);
    chain_sync(L, sync);
    if (cw == 0 && (lane & 8) == 0) {
      const f32x4 s = red4(L, lane);
      put(rs, WS_F1 + ((ks * 2 + (g >> 1)) * 512 + 16 * cg + 2 * (lane & 7) + (g & 1)) * 16, pack2(s[0], s[1]), pack2(s[2], s[3]), tg);
    }
  }
  STAMP(1);
  if (!WO_LATE) load_rows32(W, p.wo, 32 * a, cw, lane);
  gather_q(L, rs, x, ct, tg);
  if (GATEA_Q) flag_add(L, FL_GO);
  if (WO_LATE) load_rows32(W, p.wo, 32 * a, cw, lane);
  STAMP(2);
  if (cw == 0) {
    const int v = (lane >> 2) & 3, b = (lane >> 1) & 1, half = lane & 1;
    int off[1] = {WS_F3A + (((lane >> 4) * 2 + b) * 1024 + v * 256 + 2 * a + half) * 16};
    v4u gv[1];
    sweep<1, CAN_SLOW>(L, rs, off, gv, tg);
    f32x4 s = gval(gv[0]);
#pragma unroll
    for (int e = 0; e < 4; ++e) s[e] = sum_16_32(s[e]);
    const unsigned lo = pack2(s[0], s[1]), hi = pack2(s[2], s[3]);
    if (lane < 16) {
      if (v == 1 || v == 2) put(rs, WS_F3B + ((b * 2 + v - 1) * 256 + 2 * a + half) * 16, lo, hi, tg);
      else lds_st<f32x4>(L + L_GAB + ((v ? 1 : 0) * 4 + b * 2 + half) * 16, f32x4{bf_lo(lo), bf_hi(lo), bf_lo(hi), bf_hi(hi)});
    }
    STAMP(3);
    stats_exchange(L, rs, x, i, lane, tg);
  }
  STAMP(4);
  publish_partial(L, rs, x, i, ct, tg);
  STAMP(5);
  {
    const int b = ct >> 7, sl = (ct & 127) >> 3, gi = ct & 7;
    int off[2]; v4u v[2];
#pragma unroll
    for (int k = 0; k < 2; ++k) off[k] = WS_H1 + ((((b * 32 + (a >> 2)) * 4 + (a & 3)) * 32 + sl + 16 * k) * 8 + gi) * 16;
    sweep<2, CAN_TAIL>(L, rs, off, v, tg);
    STAMP(6);
    f32x4 s = gval(v[0]) + gval(v[1]);
#pragma unroll
    for (int e = 0; e < 4; ++e) { float t = s[e]; t += dpp<ROR8>(t); s[e] = sum_16_32(t); }
    if (lane < 8) {
#pragma unroll
      for (int e = 0; e < 4; ++e) lds_st<float>(L + L_RED + ((cw * 4 + e) * 8 + lane) * 4, s[e]);
    }
    WAIT_LGKM0();
    __builtin_amdgcn_s_barrier();
    const int bb = (lane >> 3) & 1;
    lptr r0 = L + L_RED + ((2 * bb * 4 + g) * 8) * 4, r1 = r0 + 128;
    const f32x4 o0 = lds_ld<f32x4>(r0) + lds_ld<f32x4>(r1), o1 = lds_ld<f32x4>(r0 + 16) + lds_ld<f32x4>(r1 + 16);
    const v4u av = {pack2(o0[0], o0[1]), pack2(o0[2], o0[3]), pack2(o1[0], o1[1]), pack2(o1[2], o1[3])};
    f32x4 acc0, acc1;
    gemv_rows32(W, dl, av, acc0, acc1);
    put_owner(rs, WS_H2, a, cw, lane, acc0, acc1, tg);
    WAIT_LGKM0();
    __builtin_amdgcn_s_barrier();
  }
  STAMP(7);
  const f32x4 attn = gather_owner<128>(L, rs, WS_H2, a, cw, lane, tg);
  STAMP(8);
  const f32x4 ga = lds_ld<f32x4>(L + L_GAB + (lane & 3) * 16), gb = lds_ld<f32x4>(L + L_GAB + (4 + (lane & 3)) * 16);
  const f32x4 xv = {bf_lo(xown.x), bf_hi(xown.x), bf_lo(xown.y), bf_hi(xown.y)};
  f32x4 x1;
#pragma unroll
  for (int e = 0; e < 4; ++e) x1[e] = xv[e] + ga[e] * rbf(attn[e]);
  const unsigned x1lo = pack2(x1[0], x1[1]), x1hi = pack2(x1[2], x1[3]);
  if (cw == 0 && lane < 4 * NREP3) put(rs, WS_H3 + (((lane >> 2) * 128 + a) * 4 + (lane & 3)) * 16, x1lo, x1hi, tg);
  STAMP(9);
  WAIT_LGKM0();
  __builtin_amdgcn_s_barrier();
  const f32x4 mlp = gather_owner<112>(L, rs, WS_H4, a, cw, lane, tg);
  STAMP(10);
  if (cw == 0 && lane < 4) {
    const f32x4 x1r = {bf_lo(x1lo), bf_hi(x1lo), bf_lo(x1hi), bf_hi(x1hi)};
    const f32x4 bv = {bf_lo(b2own.x), bf_hi(b2own.x), bf_lo(b2own.y), bf_hi(b2own.y)};
    f32x4 r;
#pragma unroll
    for (int e = 0; e < 4; ++e) r[e] = x1r[e] + gb[e] * (mlp[e] + bv[e]);
    if (*(volatile LAS unsigned*)(L + L_FLAG + FL_ABORT * 4)) r = f32x4{NAN, NAN, NAN, NAN};
    *(GAS v2u*)(p.out + (lane >> 1) * D + 8 * a + 4 * (lane & 1)) = v2u{pack2(r[0], r[1]), pack2(r[2], r[3])};
    if (a == 0 && lane == 0) __hip_atomic_store((GAS unsigned*)(p.ws + WS_EPOCH), tg.a, __ATOMIC_RELAXED, __HIP_MEMORY_SCOPE_AGENT);
  }
  STAMP(11);
}

__device__ __forceinline__ void ln_rows(f32x4 (&v)[4]) {
  float s = 0.f;
#pragma unroll
  for (int k = 0; k < 4; ++k) s += (v[k][0] + v[k][1]) + (v[k][2] + v[k][3]);
  const float mean = wave_sum(s) * (1.0f / D);
  float q = 0.f;
#pragma unroll
  for (int k = 0; k < 4; ++k) { v[k] -= mean; q += (v[k][0] * v[k][0] + v[k][1] * v[k][1]) + (v[k][2] * v[k][2] + v[k][3] * v[k][3]); }
  const float r = 1.0f / sqrtf(wave_sum(q) * (1.0f / D) + LN_EPS);
#pragma unroll
  for (int k = 0; k < 4; ++k) v[k] *= r;
}
__device__ __forceinline__ void chain_b(const Params& p, lptr L, __amdgpu_buffer_rsrc_t rs, int x, int i, int cw, int lane, Tag tg) {
  const int j = 16 * x + (i - 16), ct = cw * 64 + lane, g = lane >> 4;
  const Delta dl = make_delta(lane);
  unsigned sync = 0;
  const int ks = x & 3, cgrp = (x >> 2) * 16 + (i - 16);
  v4u W[16];
  unsigned cpair = *(const GAS unsigned*)(p.c + (lane >> 5) * D + 256 * ks + 64 * cw + 2 * (lane & 31));
  unsigned b1u = *(const GAS u16*)(p.b1 + 32 * (j < 112 ? j : 0) + (lane & 31));
  v4u xa = *(const GAS v4u*)(p.x + (cw & 1) * D + 16 * lane), xb = *(const GAS v4u*)(p.x + (cw & 1) * D + 16 * lane + 8);
  v2u xs = *(const GAS v2u*)(p.x + (cw & 1) * D + 256 * ks + 4 * lane);
  __builtin_amdgcn_sched_barrier(0);
  load_tile128(W, p.wq, 4096, 256 * ks, 128 * cgrp, cw, lane);
  asm volatile("" : "+v"(cpair), "+v"(b1u), "+v"(xa), "+v"(xb), "+v"(xs), "+v"(tg.a));
  tg.a += 1u; tg.b = ~tg.a;
  if (cw < 2) {
    const int b = cw;
    float xv[16];
#pragma unroll
    for (int e = 0; e < 4; ++e) { xv[2 * e] = bf_lo(xa[e]); xv[2 * e + 1] = bf_hi(xa[e]); xv[8 + 2 * e] = bf_lo(xb[e]); xv[9 + 2 * e] = bf_hi(xb[e]); }
    float s = 0.f;
#pragma unroll
    for (int e = 0; e < 16; ++e) s += xv[e];
    const float mean = wave_sum(s) * (1.0f / D);
    float q = 0.f;
#pragma unroll
    for (int e = 0; e < 16; ++e) { const float d = xv[e] - mean; q += d * d; }
    const float rstd = 1.0f / sqrtf(wave_sum(q) * (1.0f / D) + LN_EPS);
    if (!GATEB_Q) { asm volatile("" : "+v"(W[GOB])); flag_add(L, FL_GO); }
    int off[8]; v4u v[8];
#pragma unroll
    for (int k = 0; k < 8; ++k) off[k] = WS_F1 + (((k >> 1) * 2 + b) * 512 + (k & 1) * 256 + 64 * ks + lane) * 16;
    sweep<8, CAN_FRONT>(L, rs, off, v, tg);
    const f32x4 sa = (gval(v[0]) + gval(v[2])) + (gval(v[4]) + gval(v[6])), ca = (gval(v[1]) + gval(v[3])) + (gval(v[5]) + gval(v[7]));
    const float xs4[4] = {bf_lo(xs.x), bf_hi(xs.x), bf_lo(xs.y), bf_hi(xs.y)};
#pragma unroll
    for (int e = 0; e < 4; ++e) {
      const float h = (xs4[e] - mean) * rstd * (1.0f + rbf(ca[e])) + rbf(sa[e]);
      lds_st<u16>(L + L_ACT + (((b * 4 + (lane >> 4)) * 4 + e) * 16 + (lane & 15)) * 2, (u16)pack2(h, h));
    }
  } else if (!GATEB_Q) {
    asm volatile("" : "+v"(W[GOB]));
    flag_add(L, FL_GO);
  }
  chain_sync(L, sync);
  STAMP(1);
  {
    const f32x4 acc = gemv_tile128(L, W, dl, cw, lane);
    lds_st<f32x4>(L + L_RED + cw * 1024 + lane * 16, acc);
    chain_sync(L, sync);
    if (cw == 0) {
      const f32x4 s = red4(L, lane);
      put(rs, WS_F2 + ((ks * 2 + (g >> 1)) * 1024 + 32 * cgrp + 2 * (lane & 15) + (g & 1)) * 16, pack2(s[0], s[1]), pack2(s[2], s[3]), tg);
    }
  }
  STAMP(2);
  gather_q(L, rs, x, ct, tg);
  if (GATEB_Q) flag_add(L, FL_GO);
  STAMP(3);
  load_tile128(W, p.wmod, NMOD, 256 * ks, 2048 + 128 * cgrp, cw, lane);
  {
    const int rr = 2 * (lane & 31), b = lane >> 5;
    lptr ap = L + L_ACT + (((b * 4 + cw) * 4 + (rr & 3)) * 16 + (rr >> 2)) * 2;
    const unsigned sp = pack2(silu(bf_lo(cpair)), silu(bf_hi(cpair)));
    chain_sync(L, sync);
    lds_st<u16>(ap, (u16)sp);
    lds_st<u16>(ap + 32, (u16)(sp >> 16));
    const f32x4 acc = gemv_tile128(L, W, dl, cw, lane);
    lds_st<f32x4>(L + L_RED + cw * 1024 + lane * 16, acc);
    chain_sync(L, sync);
    if (cw == 0) {
      const f32x4 s = red4(L, lane);
      put(rs, WS_F3A + ((ks * 2 + (g >> 1)) * 1024 + 32 * cgrp + 2 * (lane & 15) + (g & 1)) * 16, pack2(s[0], s[1]), pack2(s[2], s[3]), tg);
      STAMP(4);
      stats_exchange(L, rs, x, i, lane, tg);
    }
  }
  STAMP(5);
  if (j < 112) {
    int off[4]; v4u v[4];
#pragma unroll
    for (int k = 0; k < 4; ++k) off[k] = WS_F3B + (k * 256 + ct) * 16;
    sweep<4, CAN_SLOW>(L, rs, off, v, tg);
#pragma unroll
    for (int k = 0; k < 4; ++k) lds_st<v2u>(L + L_SBCB + (k * 1024 + 4 * ct) * 2, v2u{v[k].x, v[k].z});
    flag_wait(L, FL_LASTV, 4);
    load_rows32(W, p.w2, 32 * j, cw, lane);
  }
  STAMP(6);
  publish_partial(L, rs, x, i, ct, tg);
  STAMP(7);
  if (j >= 112) return;
  if (cw < 2) {
    const int b = cw;
    int off[4]; v4u v[4];
#pragma unroll
    for (int k = 0; k < 4; ++k) off[k] = WS_H3 + ((x * 128 + (k >> 1) * 64 + lane) * 4 + 2 * b + (k & 1)) * 16;
    sweep<4, CAN_TAIL>(L, rs, off, v, tg);
    STAMP(8);
    f32x4 y[4];
#pragma unroll
    for (int k = 0; k < 4; ++k) y[k] = gval(v[k]);
    ln_rows(y);
#pragma unroll
    for (int k = 0; k < 4; ++k) {
      const int col = 8 * ((k >> 1) * 64 + lane) + 4 * (k & 1);
      const v2u sb = lds_ld<v2u>(L + L_SBCB + ((b * 2 + 0) * 1024 + col) * 2), cb = lds_ld<v2u>(L + L_SBCB + ((b * 2 + 1) * 1024 + col) * 2);
      y[k][0] = y[k][0] * (1.0f + bf_lo(cb.x)) + bf_lo(sb.x);
      y[k][1] = y[k][1] * (1.0f + bf_hi(cb.x)) + bf_hi(sb.x);
      y[k][2] = y[k][2] * (1.0f + bf_lo(cb.y)) + bf_lo(sb.y);
      y[k][3] = y[k][3] * (1.0f + bf_hi(cb.y)) + bf_hi(sb.y);
    }
    ln_rows(y);
#pragma unroll
    for (int k = 0; k < 4; ++k) {
      const int gq = 2 * ((k >> 1) * 64 + lane) + (k & 1);
      const int pos = (gq >> 3) * 32 + (gq & 3) * 8 + ((gq >> 2) & 1) * 4;
      lds_st<v2u>(L + L_Y + (b * D + pos) * 2, v2u{pack2(y[k][0], y[k][1]), pack2(y[k][2], y[k][3])});
    }
  }
  WAIT_LGKM0();
  __builtin_amdgcn_s_barrier();
  STAMP(9);
  {
    f32x4 acc[2] = {{0.f, 0.f, 0.f, 0.f}, {0.f, 0.f, 0.f, 0.f}};
    const int q = (lane & 15) >> 2, pp = lane & 3;
#pragma unroll
    for (int kk = 0; kk < 8; ++kk) {
      const v4u ya = lds_ld<v4u>(L + L_Y + ((lane & 1) * D + 256 * cw + 32 * kk + 8 * g) * 2);
#pragma unroll
      for (int ch = 0; ch < 2; ++ch) {
        v2u w[2];
#pragma unroll
        for (int hh = 0; hh < 2; ++hh) {
          const int row = (kk & 1) * 32 + 16 * hh + 4 * g + q;
          const int pc = (2 * ch + (pp >> 1)) ^ (((row >> 2) & 1) << 1);
          w[hh] = lds_tr(L + L_RING + cw * 32768 + (kk >> 1) * 4096 + row * 64 + pc * 16 + (pp & 1) * 8);
        }
        acc[ch] = mfma32(ya, v4u{w[0].x, w[0].y, w[1].x, w[1].y}, acc[ch]);
      }
    }
    if (lane < 16) {
#pragma unroll
      for (int ch = 0; ch < 2; ++ch)
#pragma unroll
        for (int b = 0; b < 2; ++b) lds_st<float>(L + L_RED + (cw * 64 + b * 32 + ch * 16 + lane) * 4, acc[ch][b]);
    }
    WAIT_LGKM0();
    __builtin_amdgcn_s_barrier();
    float a = bf_lo(b1u);
#pragma unroll
    for (int ww = 0; ww < 4; ++ww) a += lds_ld<float>(L + L_RED + (ww * 64 + lane) * 4);
    const float hv = gelu_tanh(rbf(a));
    const int k = lane & 31;
    lds_st<u16>(L + L_HID + cw * 128 + ((lane >> 5) * 32 + (k & 3) * 8 + (k >> 2)) * 2, (u16)pack2(hv, hv));
    const v4u av = lds_ld<v4u>(L + L_HID + cw * 128 + (((lane >> 3) & 1) * 32 + g * 8) * 2);
    f32x4 acc0, acc1;
    gemv_rows32(W, dl, av, acc0, acc1);
    put_owner(rs, WS_H4, j, cw, lane, acc0, acc1, tg);
  }
  STAMP(10);
}

__global__ void __launch_bounds__(512, 2) fused_decode_block(Params p) {
  extern __shared__ __attribute__((aligned(16))) unsigned char smem[];
  lptr L = (lptr)smem;
  const int tid = threadIdx.x, lane = tid & 63, wave = __builtin_amdgcn_readfirstlane(tid >> 6);
  const int x = blockIdx.x & 7, i = blockIdx.x >> 3;
  STAMP(0);
  if (tid < 16) *(LAS unsigned*)(L + L_FLAG + tid * 4) = 0u;
  WAIT_LGKM0();
  __builtin_amdgcn_s_barrier();
  if (wave < 4) {
    if (i < 16) stream_wave<false, PREW, GATE != 0 || GATEA_Q != 0>(p, L, x, i, wave, lane);
    else if (16 * x + (i - 16) < 112) stream_wave<true, PREWB, GATE != 0 || GATEB_Q != 0>(p, L, x, i, wave, lane);
    else stream_wave<false, PREWB, GATE != 0 || GATEB_Q != 0>(p, L, x, i, wave, lane);
    return;
  }
  __builtin_amdgcn_s_setprio(2);
  if (wave == 7 && i == 0) { __builtin_amdgcn_fence(__ATOMIC_RELEASE, "agent"); }
  const __amdgpu_buffer_rsrc_t rs = __builtin_amdgcn_make_buffer_rsrc((void*)p.ws, 0, WS_END, 0x00020000);
  const Tag tg = {__hip_atomic_load((GAS unsigned*)(p.ws + WS_EPOCH), __ATOMIC_RELAXED, __HIP_MEMORY_SCOPE_AGENT), 0u};
  if (i < 16) chain_a(p, L, rs, x, i, wave - 4, lane, tg);
  else chain_b(p, L, rs, x, i, wave - 4, lane, tg);
}

}

extern "C" void kernel_launch(void* const* d_in, const int* in_sizes, int n_in, void* d_out, int out_size, void* d_ws, size_t ws_size,
                              hipStream_t stream) {
  Params p;
  p.x = (const u16*)d_in[0];
  p.K = (const u16*)d_in[1];
  p.V = (const u16*)d_in[2];
  p.wq = (const u16*)d_in[3];
  p.wo = (const u16*)d_in[4];
  p.w1 = (const u16*)d_in[6];
  p.b1 = (const u16*)d_in[7];
  p.w2 = (const u16*)d_in[8];
  p.b2 = (const u16*)d_in[9];
  p.c = (const u16*)d_in[10];
  p.wmod = (const u16*)d_in[11];
  p.out = (u16*)d_out;
  p.ws = (unsigned char*)d_ws;
  (void)hipFuncSetAttribute((const void*)fused_decode_block, hipFuncAttributeMaxDynamicSharedMemorySize, LDS_BYTES);
  hipLaunchKernelGGL(fused_decode_block, dim3(256), dim3(512), LDS_BYTES, stream, p);
}
```

```cpp
#define PACE 2
#define NT_A 17
#define CAN_H1 0
#define CAN_H3 1
#define CAN_H4 1
#define DLY_H4 150
#define NPOLL 1
#define LNPOLL 1
#include <hip/hip_runtime.h>
#include <hip/hip_bf16.h>
#include <stdint.h>

#define GAS __attribute__((address_space(1)))
#define LAS __attribute__((address_space(3)))
typedef unsigned v4u __attribute__((ext_vector_type(4)));
typedef unsigned v2u __attribute__((ext_vector_type(2)));
typedef float f32x4 __attribute__((ext_vector_type(4)));
typedef __bf16 bf16x8 __attribute__((ext_vector_type(8)));
typedef __bf16 bf16x2 __attribute__((ext_vector_type(2)));
typedef short s16x4 __attribute__((ext_vector_type(4)));
typedef unsigned short u16;
typedef LAS unsigned char* lptr;

#define STAMPS 0
#define PW_A 4
#define LA_A 3
#define LA_B 4
#define LA_SM 6
#define PRE_B 0
#define CAN_QA 0
#define CAN_QB 0
#define DLY_QB 50
#define W2EARLY 0
#define NOW12 0
#define STAG 9
#define LNSTAG 6
#define WQ_DELAY 12
#define CAN_FRONT 1
#define CAN_SLOW 16
#define CAN_H2 0
#define DLY_H2 30

namespace {

constexpr int D = 1024, SK = 32768, FF = 3584, NMOD = 6144;
constexpr float SC_LOG2 = 0.08838834764831845f * 1.4426950408889634f;
constexpr float LN_EPS = 1e-5f;

constexpr int WS_EPOCH = 0;
constexpr int WS_F1  = 4096;
constexpr int WS_F2  = WS_F1 + 4096 * 16;
constexpr int WS_F3A = WS_F2 + 8192 * 16;
constexpr int WS_F3B = WS_F3A + 8192 * 16;
constexpr int WS_ST  = WS_F3B + 1024 * 16;
constexpr int WS_H1  = WS_ST + 2048 * 16;
constexpr int WS_H2  = WS_H1 + 65536 * 16;
constexpr int NREP3  = 8;
constexpr int WS_H3  = WS_H2 + 65536 * 16;
constexpr int WS_H4  = WS_H3 + NREP3 * 1024 * 16;
constexpr int WS_END = WS_H4 + 65536 * 16;
constexpr int WS_STAMP = 8 << 20;

constexpr int L_RING = 0;
constexpr int L_MISC = 131072;
constexpr int L_ACT  = L_MISC;
constexpr int L_Q    = L_ACT + 1024;
constexpr int L_RED  = L_Q + 2048;
constexpr int L_SBCB = L_RED + 4096;
constexpr int L_Y    = L_SBCB + 8192;
constexpr int L_STAT = L_Y + 4096;
constexpr int L_FAC  = L_STAT + 256;
constexpr int L_GAB  = L_FAC + 128;
constexpr int L_HID  = L_GAB + 128;
constexpr int L_RED2 = L_HID + 512;
constexpr int L_FLAG = L_RED2 + 1024;
constexpr int LDS_BYTES = 163840;
static_assert(L_FLAG + 64 <= LDS_BYTES, "LDS map");
enum { FL_SYNC = 0, FL_Q = 1, FL_STAT = 2, FL_FAC = 3, FL_ODONE = 4, FL_LASTV = 5, FL_ABORT = 6, FL_GO = 7, FL_RED = 8, FL_GO0 = 9, FL_X1 = 10, FL_OUT = 11, FL_LN0 = 12, FL_LN1 = 13 };

constexpr unsigned SPIN_GLOBAL = 1u << 16;
constexpr unsigned SPIN_LDS = 1u << 21;

struct Params {
  const u16 *x, *K, *V, *wq, *wo, *w1, *b1, *w2, *b2, *c, *wmod;
  u16* out;
  unsigned char* ws;
};

#define PIN(x) asm volatile("" : "+v"(x))
__device__ __forceinline__ float bf_lo(unsigned u) { return __builtin_bit_cast(float, u << 16); }
__device__ __forceinline__ float bf_hi(unsigned u) { return __builtin_bit_cast(float, u & 0xffff0000u); }
__device__ __forceinline__ unsigned pack2(float lo, float hi) { bf16x2 r = {(__bf16)lo, (__bf16)hi}; return __builtin_bit_cast(unsigned, r); }
__device__ __forceinline__ float rbf(float v) { return bf_lo(pack2(v, v)); }
template <int CTRL> __device__ __forceinline__ float dpp(float v) {
  return __builtin_bit_cast(float, __builtin_amdgcn_mov_dpp(__builtin_bit_cast(int, v), CTRL, 0xf, 0xf, true));
}
constexpr int XOR1 = 0xB1, XOR2 = 0x4E, ROR4 = 0x124, ROR8 = 0x128;
__device__ __forceinline__ float sum_16_32(float v) {
  auto s = __builtin_amdgcn_permlane16_swap(__float_as_uint(v), __float_as_uint(v), false, false);
  v = __uint_as_float(s[0]) + __uint_as_float(s[1]);
  auto t = __builtin_amdgcn_permlane32_swap(__float_as_uint(v), __float_as_uint(v), false, false);
  return __uint_as_float(t[0]) + __uint_as_float(t[1]);
}
__device__ __forceinline__ float max_16_32(float v) {
  auto s = __builtin_amdgcn_permlane16_swap(__float_as_uint(v), __float_as_uint(v), false, false);
  v = fmaxf(__uint_as_float(s[0]), __uint_as_float(s[1]));
  auto t = __builtin_amdgcn_permlane32_swap(__float_as_uint(v), __float_as_uint(v), false, false);
  return fmaxf(__uint_as_float(t[0]), __uint_as_float(t[1]));
}
__device__ __forceinline__ void wave_sum2(float& a, float& b) {
  a += dpp<XOR1>(a); b += dpp<XOR1>(b);
  a += dpp<XOR2>(a); b += dpp<XOR2>(b);
  a += dpp<ROR4>(a); b += dpp<ROR4>(b);
  a += dpp<ROR8>(a); b += dpp<ROR8>(b);
  a = sum_16_32(a); b = sum_16_32(b);
}
__device__ __forceinline__ f32x4 mfma32(v4u a, v4u b, f32x4 c) {
  return __builtin_amdgcn_mfma_f32_16x16x32_bf16(__builtin_bit_cast(bf16x8, a), __builtin_bit_cast(bf16x8, b), c, 0, 0, 0);
}
__device__ __forceinline__ f32x4 mfma16(v2u a, v2u b, f32x4 c) {
  return __builtin_amdgcn_mfma_f32_16x16x16bf16_1k(__builtin_bit_cast(s16x4, a), __builtin_bit_cast(s16x4, b), c, 0, 0, 0);
}
__device__ __forceinline__ v2u lds_tr(lptr p) {
  return __builtin_bit_cast(v2u, __builtin_amdgcn_ds_read_tr16_b64_v4i16((LAS s16x4*)p));
}
template <class T> __device__ __forceinline__ T lds_ld(lptr p) { return *(LAS T*)p; }
template <class T> __device__ __forceinline__ void lds_st(lptr p, T v) { *(LAS T*)p = v; }
__device__ __forceinline__ v4u ldw(const u16* p) { return __builtin_nontemporal_load((const GAS v4u*)p); }
#define WAIT_VM(n) asm volatile("s_waitcnt vmcnt(%0)" ::"i"(n) : "memory")
#define WAIT_LGKM0() asm volatile("s_waitcnt lgkmcnt(0)" ::: "memory")
__device__ __forceinline__ unsigned long long now10ns() { return __builtin_amdgcn_s_memrealtime(); }

#define SSTAMP(idx)
#define SSTAMP_DECL
#define SSTAMP_FLUSH()

__device__ __forceinline__ void flag_add(lptr L, int f) {
  unsigned addr = (unsigned)(size_t)L + L_FLAG + f * 4;
  unsigned one = 1u;
  unsigned long long keep;
  asm volatile("s_waitcnt lgkmcnt(0)\n\ts_mov_b64 %0, exec\n\ts_mov_b64 exec, 1\n\tds_add_u32 %1, %2\n\ts_mov_b64 exec, %0"
               : "=&s"(keep) : "v"(addr), "v"(one) : "memory");
}
__device__ __forceinline__ bool flag_claim(lptr L, int f) {
  unsigned old = 0;
  if ((threadIdx.x & 63) == 0) old = __hip_atomic_fetch_add((LAS unsigned*)(L + L_FLAG) + f, 1u, __ATOMIC_RELAXED, __HIP_MEMORY_SCOPE_WORKGROUP);
  return __builtin_amdgcn_readfirstlane(old) == 0u;
}
__device__ __forceinline__ void wg_barrier() { WAIT_LGKM0(); __builtin_amdgcn_s_barrier(); asm volatile("" ::: "memory"); }
template <int SLEEP>
__device__ __forceinline__ void flag_wait(lptr L, int f, unsigned want) {
  volatile LAS unsigned* fl = (volatile LAS unsigned*)(L + L_FLAG);
#pragma nounroll
  for (unsigned sp = 0; fl[f] < want; ++sp) {
    if (sp > SPIN_LDS) { fl[FL_ABORT] = 1; break; }
    if (SLEEP) __builtin_amdgcn_s_sleep(SLEEP);
  }
  asm volatile("" ::: "memory");
}

struct Tag { unsigned a, b; };
__device__ __forceinline__ void put(__amdgpu_buffer_rsrc_t rs, int off, unsigned lo, unsigned hi, Tag t) {
  v4u g = {lo, t.a, hi, t.b};
  __builtin_amdgcn_raw_buffer_store_b128(g, rs, off, 0, 16);
}
__device__ __forceinline__ void put4(__amdgpu_buffer_rsrc_t rs, int off, f32x4 v, Tag t) { put(rs, off, pack2(v[0], v[1]), pack2(v[2], v[3]), t); }
template <int NL, int CAN>
__device__ __forceinline__ void sweep(lptr L, __amdgpu_buffer_rsrc_t rs, const int (&off)[NL], v4u (&v)[NL], Tag t) {
  if (CAN > 0) {
    const int oc = __builtin_amdgcn_readlane(off[NL - 1], 63);
#pragma nounroll
    for (unsigned sp = 0;; ++sp) {
      const v4u c = __builtin_amdgcn_raw_buffer_load_b128(rs, 0, oc, 16);
      if (__all((c.y == t.a) & (c.w == t.b))) break;
      if (sp > SPIN_GLOBAL) break;
      if (CAN > 1) __builtin_amdgcn_s_sleep(CAN - 1);
      asm volatile("" ::: "memory");
    }
  }
#pragma nounroll
  for (unsigned sp = 0;; ++sp) {
#pragma unroll
    for (int k = 0; k < NL; ++k) v[k] = __builtin_amdgcn_raw_buffer_load_b128(rs, off[k], 0, 16);
    bool ok = true;
#pragma unroll
    for (int k = 0; k < NL; ++k) ok &= (v[k].y == t.a) & (v[k].w == t.b);
    if (__all(ok)) break;
    if (sp > SPIN_GLOBAL) { *(volatile LAS unsigned*)(L + L_FLAG + FL_ABORT * 4) = 1; break; }
    asm volatile("" ::: "memory");
  }
}
__device__ __forceinline__ void delay_since(unsigned long long t0, int ticks) {
  if (ticks > 0) { while (now10ns() - t0 < (unsigned long long)ticks) {} }
}
__device__ __forceinline__ f32x4 gval(v4u g) { return f32x4{bf_lo(g.x), bf_hi(g.x), bf_lo(g.z), bf_hi(g.z)}; }
__device__ __forceinline__ v2u sel_op(int lane) {
  const int m = lane & 3;
  return v2u{m == 0 ? 0x00003f80u : m == 1 ? 0x3f800000u : 0u, m == 2 ? 0x00003f80u : m == 3 ? 0x3f800000u : 0u};
}
__device__ __forceinline__ f32x4 gsum(v2u sel, v4u g, f32x4 acc) { return mfma16(sel, v2u{g.x, g.z}, acc); }
__device__ __forceinline__ f32x4 gsum_t(v2u sel, v4u g, f32x4 acc) { return mfma16(v2u{g.x, g.z}, sel, acc); }

struct Delta { unsigned m[4]; };
__device__ __forceinline__ Delta make_delta(int lane) {
  const int j = lane & 7;
  Delta d;
#pragma unroll
  for (int r = 0; r < 4; ++r) { d.m[r] = (r == (j >> 1)) ? ((j & 1) ? 0xffff0000u : 0x0000ffffu) : 0u; PIN(d.m[r]); }
  return d;
}
__device__ __forceinline__ v4u delta_op(const Delta& d, unsigned pair, int hi) {
  const unsigned dup = __builtin_amdgcn_perm(pair, pair, hi ? 0x03020302u : 0x01000100u);
  return v4u{dup & d.m[0], dup & d.m[1], dup & d.m[2], dup & d.m[3]};
}

#define DMA_TILE(o0, o1, o2, o3, base, ldsaddr)                                                          \
  do {                                                                                                   \
    unsigned keep_;                                                                                      \
    asm volatile("s_nop 4\n\ts_mov_b32 %0, m0\n\ts_mov_b32 m0, %6\n\ts_nop 0\n\t"                        \
                 "global_load_lds_dwordx4 %1, %5 nt\n\t"                                                 \
                 "global_load_lds_dwordx4 %2, %5 offset:1024 nt\n\t"                                     \
                 "global_load_lds_dwordx4 %3, %5 offset:2048 nt\n\t"                                     \
                 "global_load_lds_dwordx4 %4, %5 offset:3072 nt\n\t"                                     \
                 "s_mov_b32 m0, %0"                                                                      \
                 : "=&s"(keep_)                                                                          \
                 : "v"(o0), "v"(o1), "v"(o2), "v"(o3), "s"(base), "s"(ldsaddr)                           \
                 : "memory");                                                                            \
  } while (0)

constexpr int last_issued(int t, int npre, int la, int nt) {
  int li = npre - 1;
  for (int s = 0; s <= t; ++s) {
    int ahead = la;
    if (s >= nt - 3 && s <= nt - 1) ahead = LA_SM > la ? LA_SM : la;
    int want = s + ahead;
    if (npre > la) {
      const int w2 = s + 8 < npre - 1 + la ? s + 8 : npre - 1 + la;
      if (w2 > want) want = w2;
    }
    if (want > s + 8) want = s + 8;
    if (want > 2 * nt - 1) want = 2 * nt - 1;
    if (want > li) li = want;
  }
  return li;
}
template <int NT, int NPRE, int PW, int LA, int NBAR>
__device__ __forceinline__ void stream_wave(const Params& p, lptr L, int x, int i, int w, int lane) {
  SSTAMP_DECL;
  const int g = lane >> 4, n = lane & 15;
  const size_t key0 = (size_t)x * SK + (i < 16 ? 64 * NT_A * i : 1024 * NT_A + 64 * (32 - NT_A) * (i - 16)) + 16 * NT * w;
  const char* Kb = (const char*)p.K + key0 * 256;
  const char* Vb = (const char*)p.V + key0 * 256;
  const unsigned ring = (unsigned)(size_t)L + L_RING + w * 32768;
  unsigned voK[4], voV[2];
#pragma unroll
  for (int v = 0; v < 4; ++v) voK[v] = g * 256 + ((n ^ (4 * v + g)) << 4);
#pragma unroll
  for (int v = 0; v < 2; ++v) voV[v] = g * 256 + ((n ^ (((4 * v + g) & 7) << 1)) << 4);

#define ISSUE(t)                                                                                                \
  do {                                                                                                          \
    const unsigned la_ = ring + ((t)&7) * 4096;                                                                 \
    if ((t) < NT) { const char* b_ = Kb + (t)*4096; DMA_TILE(voK[0], voK[1], voK[2], voK[3], b_, la_); }         \
    else { const char* b_ = Vb + ((t)-NT) * 4096; DMA_TILE(voV[0], voV[1], voV[0], voV[1], b_, la_); }          \
  } while (0)
#define LI(t) last_issued(t, NPRE, LA, NT)
#define REFILL(t)                                                                       \
  do {                                                                                  \
    _Pragma("unroll") for (int u_ = LI((t)-1) + 1; u_ <= LI(t); ++u_) ISSUE(u_);          \
  } while (0)

#pragma unroll
  for (int k = 0; k < NBAR; ++k) __builtin_amdgcn_s_barrier();
  SSTAMP(7);
#pragma unroll
  for (int t = 0; t < PW; ++t) ISSUE(t);
#pragma unroll
  for (int t = PW; t < NPRE; ++t) { WAIT_VM(4 * (PW - 1)); ISSUE(t); }
  SSTAMP(1);
  if (NBAR == 1) __builtin_amdgcn_s_barrier();
  asm volatile("" ::: "memory");
  SSTAMP(2);

  v4u qf[4];
#pragma unroll
  for (int t4 = 0; t4 < 4; ++t4) qf[t4] = lds_ld<v4u>(L + L_Q + (n & 7) * 256 + t4 * 64 + g * 16);
  lptr rb = L + L_RING + w * 32768;
  int ka[4];
#pragma unroll
  for (int t4 = 0; t4 < 4; ++t4) ka[t4] = n * 256 + (((4 * t4 + g) ^ n) << 4);

  f32x4 sc[NT];
#pragma unroll
  for (int t = 0; t < NT; ++t) {
    WAIT_VM(4 * (LI(t - 1) - t));
    v4u kf[4];
#pragma unroll
    for (int t4 = 0; t4 < 4; ++t4) kf[t4] = lds_ld<v4u>(rb + (t & 7) * 4096 + ka[t4]);
    WAIT_LGKM0();
    REFILL(t);
    f32x4 a = {0.f, 0.f, 0.f, 0.f};
#pragma unroll
    for (int t4 = 0; t4 < 4; ++t4) a = mfma32(kf[t4], qf[t4], a);
    sc[t] = a;
  }
  SSTAMP(3);
  float m = -INFINITY;
#pragma unroll
  for (int t = 0; t < NT; ++t) m = fmaxf(fmaxf(fmaxf(sc[t][0], sc[t][1]), fmaxf(sc[t][2], sc[t][3])), m);
  m = max_16_32(m);
  const float mm = m * SC_LOG2;
  float lsum = 0.f;
  v2u pb[NT];
#pragma unroll
  for (int t = 0; t < NT; ++t) {
    float e[4];
#pragma unroll
    for (int r = 0; r < 4; ++r) { e[r] = __builtin_amdgcn_exp2f(fmaf(sc[t][r], SC_LOG2, -mm)); lsum += e[r]; }
    pb[t] = v2u{pack2(e[0], e[1]), pack2(e[2], e[3])};
  }
  lsum = sum_16_32(lsum);
  if (lane < 8) lds_st<v2u>(L + L_STAT + (w * 8 + lane) * 8, v2u{__float_as_uint(mm), __float_as_uint(lsum)});
  flag_add(L, FL_STAT);

  int va[8];
  {
    const int q = (lane & 15) >> 2, pp = lane & 3, row = 4 * g + q;
#pragma unroll
    for (int u = 0; u < 8; ++u) va[u] = row * 256 + (((2 * u + (pp >> 1)) ^ ((row & 7) << 1)) << 4) + (pp & 1) * 8;
  }
  f32x4 o[8];
#pragma unroll
  for (int u = 0; u < 8; ++u) o[u] = f32x4{0.f, 0.f, 0.f, 0.f};
#pragma unroll
  for (int t = NT; t < 2 * NT; ++t) {
    WAIT_VM(4 * (LI(t - 1) - t));
    v2u vf[8];
#pragma unroll
    for (int u = 0; u < 8; ++u) vf[u] = lds_tr(rb + (t & 7) * 4096 + va[u]);
    WAIT_LGKM0();
    REFILL(t);
#pragma unroll
    for (int u = 0; u < 8; ++u) o[u] = mfma16(vf[u], pb[t - NT], o[u]);
  }
  SSTAMP(4);
  if (n < 8) {
#pragma unroll
    for (int u = 0; u < 8; ++u) lds_st<f32x4>(rb + 4 * 4096 + n * 528 + (16 * u + 4 * g) * 4, o[u]);
  }
  flag_add(L, FL_ODONE);
  SSTAMP(5);
  SSTAMP_FLUSH();
#undef ISSUE
#undef LI
#undef REFILL
}

__device__ __forceinline__ f32x4 gemv_tile128(lptr act, const v4u (&W)[16], const Delta& dl, int cw, int lane) {
  const int g = lane >> 4, b = (lane >> 3) & 1;
  const v4u a0 = lds_ld<v4u>(act + ((b * 4 + cw) * 4 + g) * 32), a1 = lds_ld<v4u>(act + ((b * 4 + cw) * 4 + g) * 32 + 16);
  f32x4 acc0 = {0.f, 0.f, 0.f, 0.f}, acc1 = acc0;
#pragma unroll
  for (int u = 0; u < 8; ++u) {
    acc0 = mfma32(delta_op(dl, a0[u >> 1], u & 1), W[u], acc0);
    acc1 = mfma32(delta_op(dl, a1[u >> 1], u & 1), W[8 + u], acc1);
  }
  return acc0 + acc1;
}
__device__ __forceinline__ f32x4 reduce_to_wave0(lptr L, int cw, int lane, f32x4 acc, unsigned want) {
  if (cw != 0) { lds_st<f32x4>(L + L_RED + cw * 1024 + lane * 16, acc); flag_add(L, FL_RED); return acc; }
  flag_wait<0>(L, FL_RED, want);
#pragma unroll
  for (int ww = 1; ww < 4; ++ww) acc += lds_ld<f32x4>(L + L_RED + ww * 1024 + lane * 16);
  return acc;
}
__device__ __forceinline__ void gemv_rows32(const v4u (&W)[16], const Delta& dl, v4u av, f32x4& acc0, f32x4& acc1) {
  acc0 = f32x4{0.f, 0.f, 0.f, 0.f}; acc1 = acc0;
#pragma unroll
  for (int u = 0; u < 8; ++u) {
    const v4u a = delta_op(dl, av[u >> 1], u & 1);
    acc0 = mfma32(a, W[u], acc0);
    acc1 = mfma32(a, W[8 + u], acc1);
  }
}
__device__ __forceinline__ void load_rows32(v4u (&W)[16], const u16* Wg, int row0, int cw, int lane) {
  const int g = lane >> 4, c = lane & 15;
#pragma unroll
  for (int cb = 0; cb < 2; ++cb)
#pragma unroll
    for (int u = 0; u < 8; ++u) W[cb * 8 + u] = ldw(Wg + (size_t)(row0 + 4 * u + g) * D + (2 * cw + cb) * 128 + 8 * c);
  __builtin_amdgcn_sched_barrier(0);
}
__device__ __forceinline__ void load_tile128(v4u (&W)[16], const u16* Wg, int ld, int row0, int col0, int cw, int lane) {
  const int g = lane >> 4, c = lane & 15;
#pragma unroll
  for (int u = 0; u < 16; ++u) W[u] = ldw(Wg + (size_t)(row0 + 64 * cw + 4 * u + g) * ld + col0 + 8 * c);
  __builtin_amdgcn_sched_barrier(0);
}
__device__ __forceinline__ void put_owner(__amdgpu_buffer_rsrc_t rs, int base, int prod, int cw, int lane, f32x4 acc0, f32x4 acc1, Tag tg) {
  const int o0 = base + prod * 8192 + (32 * cw + (lane & 15)) * 64 + (lane >> 4) * 16;
  put4(rs, o0, acc0, tg);
  put4(rs, o0 + 1024, acc1, tg);
}
template <int NP, int CAN>
__device__ __forceinline__ f32x4 gather_owner(lptr L, __amdgpu_buffer_rsrc_t rs, int off0, v2u sel, Tag tg) {
  constexpr int NL = NP / 16;
  int off[NL]; v4u v[NL];
#pragma unroll
  for (int k = 0; k < NL; ++k) off[k] = off0 + k * 16 * 8192;
  sweep<NL, CAN>(L, rs, off, v, tg);
  f32x4 s = {0.f, 0.f, 0.f, 0.f};
#pragma unroll
  for (int k = 0; k < NL; ++k) s = gsum(sel, v[k], s);
#pragma unroll
  for (int e = 0; e < 4; ++e) { float a = s[e]; a += dpp<ROR4>(a); s[e] = a + dpp<ROR8>(a); }
  return s;
}
__device__ __forceinline__ float silu(float v) { return v * __builtin_amdgcn_rcpf(1.0f + __expf(-v)); }
__device__ __forceinline__ float gelu_tanh(float a) {
  const float u = 0.7978845608028654f * (a + 0.044715f * a * a * a);
  const float e = __expf(2.0f * u);
  const float th = 1.0f - 2.0f * __builtin_amdgcn_rcpf(e + 1.0f);
  return 0.5f * a * (1.0f + th);
}

template <int CAN>
__device__ __forceinline__ void gather_q(lptr L, __amdgpu_buffer_rsrc_t rs, const int (&off)[4], int ct, Tag tg) {
  v4u v[4];
  sweep<4, CAN>(L, rs, off, v, tg);
  const f32x4 s = (gval(v[0]) + gval(v[1])) + (gval(v[2]) + gval(v[3]));
  lds_st<v2u>(L + L_Q + ct * 8, v2u{pack2(s[0], s[1]), pack2(s[2], s[3])});
  wg_barrier();
}
__device__ __forceinline__ void q_offsets(int (&off)[4], int x, int ct) {
#pragma unroll
  for (int k = 0; k < 4; ++k) { off[k] = WS_F2 + ((k * 2 + (x >> 2)) * 1024 + (x & 3) * 256 + ct) * 16; PIN(off[k]); }
}
__device__ __forceinline__ void stats_exchange(lptr L, __amdgpu_buffer_rsrc_t rs, int x, int i, int lane, Tag tg) {
  flag_wait<4>(L, FL_STAT, 4);
  const int h = lane & 7;
  float mw[4], lw[4];
#pragma unroll
  for (int w = 0; w < 4; ++w) { const v2u s = lds_ld<v2u>(L + L_STAT + (w * 8 + h) * 8); mw[w] = __uint_as_float(s.x); lw[w] = __uint_as_float(s.y); }
  const float mc = fmaxf(fmaxf(mw[0], mw[1]), fmaxf(mw[2], mw[3]));
  float lc = 0.f;
#pragma unroll
  for (int w = 0; w < 4; ++w) lc += lw[w] * __builtin_amdgcn_exp2f(mw[w] - mc);
  if (lane < 8) put(rs, WS_ST + ((x * 32 + i) * 8 + h) * 16, __float_as_uint(mc), __float_as_uint(lc), tg);
  int off[4]; v4u v[4];
#pragma unroll
  for (int k = 0; k < 4; ++k) off[k] = WS_ST + ((x * 32 + (lane >> 3) + 8 * k) * 8 + h) * 16;
  sweep<4, CAN_SLOW>(L, rs, off, v, tg);
  float M = -INFINITY;
#pragma unroll
  for (int k = 0; k < 4; ++k) M = fmaxf(M, __uint_as_float(v[k].x));
  M = fmaxf(M, dpp<ROR8>(M));
  M = max_16_32(M);
  float S = 0.f;
#pragma unroll
  for (int k = 0; k < 4; ++k) S += __uint_as_float(v[k].z) * __builtin_amdgcn_exp2f(__uint_as_float(v[k].x) - M);
  S += dpp<ROR8>(S);
  S = sum_16_32(S);
  const float inv = 1.0f / S;
  if (lane < 32) {
    const int w = lane >> 3;
    const float mws = w == 0 ? mw[0] : w == 1 ? mw[1] : w == 2 ? mw[2] : mw[3];
    lds_st<float>(L + L_FAC + lane * 4, __builtin_amdgcn_exp2f(mws - M) * inv);
  }
  flag_add(L, FL_FAC);
}
__device__ __forceinline__ void publish_partial(lptr L, __amdgpu_buffer_rsrc_t rs, int x, int i, int ct, Tag tg) {
  const int h = ct >> 5, gi = ct & 31;
  int off = WS_H1 + (((((x >> 2) * 32 + (x & 3) * 8 + h) * 4 + (gi >> 3)) * 32 + i) * 8 + (gi & 7)) * 16;
  PIN(off);
  flag_wait<2>(L, FL_FAC, 1);
  float f[4];
#pragma unroll
  for (int w = 0; w < 4; ++w) { f[w] = lds_ld<float>(L + L_FAC + (w * 8 + h) * 4); PIN(f[w]); }
  flag_wait<0>(L, FL_ODONE, 4);
  f32x4 s = lds_ld<f32x4>(L + L_RING + 4 * 4096 + h * 528 + gi * 16) * f[0];
#pragma unroll
  for (int w = 1; w < 4; ++w) s += lds_ld<f32x4>(L + L_RING + w * 32768 + 4 * 4096 + h * 528 + gi * 16) * f[w];
  put4(rs, off, s, tg);
}

__device__ __forceinline__ void chain_a(const Params& p, lptr L, __amdgpu_buffer_rsrc_t rs, int x, int i, int cw, int lane) {
  SSTAMP_DECL;
  const int a = 16 * x + i, ct = cw * 64 + lane, g = lane >> 4;
  const int cg = a >> 2, ks = a & 3, row0 = 256 * ks + 64 * cw;
  v4u W[16];
  unsigned cpair = *(const GAS unsigned*)(p.c + (lane >> 5) * D + row0 + 2 * (lane & 31));
  __builtin_amdgcn_sched_barrier(0);
#pragma unroll
  for (int u = 0; u < 8; ++u) W[u] = ldw(p.wmod + (size_t)(row0 + 8 * u + (lane >> 3)) * NMOD + 64 * cg + 8 * (lane & 7));
  __builtin_amdgcn_sched_barrier(0);
  Tag tg = {__hip_atomic_load((GAS unsigned*)(p.ws + WS_EPOCH), __ATOMIC_RELAXED, __HIP_MEMORY_SCOPE_AGENT), 0u};
  __builtin_amdgcn_sched_barrier(0);
  SSTAMP(12);
  const Delta dl = make_delta(lane);
  unsigned spair = pack2(silu(bf_lo(cpair)), silu(bf_hi(cpair)));
  PIN(spair);
  {
    int foff = WS_F1 + ((ks * 2 + (g >> 1)) * 512 + 16 * cg + 2 * (lane & 7) + (g & 1)) * 16;
    PIN(foff);
    const int rr = 2 * (lane & 31), b = lane >> 5;
    lds_st<unsigned>(L + L_ACT + cw * 256 + (b * 64 + ((rr >> 1) & 3) * 16 + (rr >> 3) * 2) * 2, spair);
    const int bb = (lane >> 3) & 1;
    const v4u a0 = lds_ld<v4u>(L + L_ACT + cw * 256 + (bb * 64 + g * 16) * 2), a1 = lds_ld<v4u>(L + L_ACT + cw * 256 + (bb * 64 + g * 16) * 2 + 16);
    v4u da[8][2];
#pragma unroll
    for (int u = 0; u < 8; ++u) {
      const unsigned pr = u < 4 ? a0[u] : a1[u - 4];
      da[u][0] = delta_op(dl, pr, 0); da[u][1] = delta_op(dl, pr, 1);
      PIN(da[u][0]); PIN(da[u][1]);
    }
    SSTAMP(13);
    f32x4 acc0 = {0.f, 0.f, 0.f, 0.f}, acc1 = acc0;
#pragma unroll
    for (int u = 0; u < 8; ++u) {
      acc0 = mfma32(da[u][0], W[u], acc0);
      acc1 = mfma32(da[u][1], W[u], acc1);
    }
    f32x4 t = (lane & 8) ? acc1 : acc0;
#pragma unroll
    for (int e = 0; e < 4; ++e) t[e] += dpp<ROR8>(t[e]);
    if (cw != 0) lds_st<f32x4>(L + L_RED + cw * 1024 + lane * 16, t);
    SSTAMP(14);
    wg_barrier();
    PIN(tg.a);
    tg.a += 1u; tg.b = ~tg.a;
    if (cw == 0) {
#pragma unroll
      for (int ww = 1; ww < 4; ++ww) t += lds_ld<f32x4>(L + L_RED + ww * 1024 + lane * 16);
      if ((lane & 8) == 0) put4(rs, foff, t, tg);
    }
  }
  SSTAMP(1);
  v2u xown = *(const GAS v2u*)(p.x + ((lane >> 1) & 1) * D + 8 * a + 4 * (lane & 1));
  v2u b2own = *(const GAS v2u*)(p.b2 + 8 * a + 4 * (lane & 1));
  __builtin_amdgcn_sched_barrier(0);
  v4u W2[16];
  load_tile128(W2, p.wmod, NMOD, 256 * ks, 2048 + 128 * cg, cw, lane);
  load_rows32(W, p.wo, 32 * a, cw, lane);
  PIN(xown); PIN(b2own);
  {
    const int rr = 2 * (lane & 31), bb = lane >> 5;
    lptr ap = L + L_Y + (((bb * 4 + cw) * 4 + (rr & 3)) * 16 + (rr >> 2)) * 2;
    lds_st<u16>(ap, (u16)spair);
    lds_st<u16>(ap + 32, (u16)(spair >> 16));
  }
  { int offq[4]; q_offsets(offq, x, ct); gather_q<CAN_QA>(L, rs, offq, ct, tg); }
  SSTAMP(2);
  {
    const f32x4 s = reduce_to_wave0(L, cw, lane, gemv_tile128(L + L_Y, W2, dl, cw, lane), 3);
    if (cw == 0) put4(rs, WS_F3A + ((ks * 2 + (g >> 1)) * 1024 + 32 * cg + 2 * (lane & 15) + (g & 1)) * 16, s, tg);
  }
  if (cw == 0) {
    const int v = (lane >> 2) & 3, b = (lane >> 1) & 1, half = lane & 1;
    int off[1] = {WS_F3A + (((lane >> 4) * 2 + b) * 1024 + v * 256 + 2 * a + half) * 16};
    v4u gv[1];
    sweep<1, CAN_SLOW>(L, rs, off, gv, tg);
    f32x4 s = gval(gv[0]);
#pragma unroll
    for (int e = 0; e < 4; ++e) s[e] = sum_16_32(s[e]);
    const unsigned lo = pack2(s[0], s[1]), hi = pack2(s[2], s[3]);
    if (lane < 16) {
      if (v == 1 || v == 2) put(rs, WS_F3B + ((b * 2 + v - 1) * 256 + 2 * a + half) * 16, lo, hi, tg);
      else lds_st<f32x4>(L + L_GAB + ((v ? 1 : 0) * 4 + b * 2 + half) * 16, f32x4{bf_lo(lo), bf_hi(lo), bf_lo(hi), bf_hi(hi)});
    }
    SSTAMP(3);
    stats_exchange(L, rs, x, i, lane, tg);
  }
  SSTAMP(4);
  int off1[8];
#pragma unroll
  for (int k = 0; k < 8; ++k) { off1[k] = WS_H1 + ((((((lane >> 3) & 1) * 32 + (a >> 2)) * 4 + (a & 3)) * 32 + 4 * k + g) * 8 + (lane & 7)) * 16; PIN(off1[k]); }
  int bp0 = (32 * ((lane >> 3) & 1) + g) * 4, bp1 = bp0 + 64;
  int off2 = WS_H2 + (lane >> 2) * 8192 + a * 64 + (lane & 3) * 16, off4 = off2 + (WS_H4 - WS_H2);
  PIN(bp0); PIN(bp1); PIN(off2); PIN(off4);
  v2u sel = sel_op(lane); PIN(sel);
  publish_partial(L, rs, x, i, ct, tg);
  SSTAMP(5);
  unsigned long long tpub;
  {
    v4u v[8];
    sweep<8, CAN_H1>(L, rs, off1, v, tg);
    SSTAMP(6);
    f32x4 s = {0.f, 0.f, 0.f, 0.f};
#pragma unroll
    for (int k = 0; k < 8; ++k) s = gsum_t(sel, v[k], s);
    const unsigned p01 = pack2(s[0], s[1]), p23 = pack2(s[2], s[3]);
    const v4u av = {(unsigned)__builtin_amdgcn_ds_bpermute(bp0, (int)p01), (unsigned)__builtin_amdgcn_ds_bpermute(bp0, (int)p23),
                    (unsigned)__builtin_amdgcn_ds_bpermute(bp1, (int)p01), (unsigned)__builtin_amdgcn_ds_bpermute(bp1, (int)p23)};
    f32x4 acc0, acc1;
    gemv_rows32(W, dl, av, acc0, acc1);
    put_owner(rs, WS_H2, a, cw, lane, acc0, acc1, tg);
    tpub = now10ns();
  }
  SSTAMP(7);
  if (cw < NPOLL) {
    const f32x4 ga = lds_ld<f32x4>(L + L_GAB + (lane & 3) * 16), gb = lds_ld<f32x4>(L + L_GAB + (4 + (lane & 3)) * 16);
    const f32x4 xv = {bf_lo(xown.x), bf_hi(xown.x), bf_lo(xown.y), bf_hi(xown.y)};
    int off3 = WS_H3 + (((lane >> 2) * 128 + a) * 8 + (lane & 3)) * 16;
    PIN(off3);
    delay_since(tpub, DLY_H2 + cw * STAG);
    const f32x4 attn = gather_owner<128, CAN_H2>(L, rs, off2, sel, tg);
    SSTAMP(8);
    f32x4 x1;
#pragma unroll
    for (int e = 0; e < 4; ++e) x1[e] = xv[e] + ga[e] * rbf(attn[e]);
    const unsigned x1lo = pack2(x1[0], x1[1]), x1hi = pack2(x1[2], x1[3]);
    if (NPOLL == 1 || flag_claim(L, FL_X1)) { if (lane < 4 * NREP3) put(rs, off3, x1lo, x1hi, tg); }
    tpub = now10ns();
    SSTAMP(9);
    const f32x4 x1r = {bf_lo(x1lo), bf_hi(x1lo), bf_lo(x1hi), bf_hi(x1hi)};
    const f32x4 bv = {bf_lo(b2own.x), bf_hi(b2own.x), bf_lo(b2own.y), bf_hi(b2own.y)};
    if (cw == 0) {
      delay_since(tpub, DLY_H4);
      const f32x4 mlp = gather_owner<112, CAN_H4>(L, rs, off4, sel, tg);
      SSTAMP(10);
      if (lane < 4) {
        f32x4 r;
#pragma unroll
        for (int e = 0; e < 4; ++e) r[e] = x1r[e] + gb[e] * (mlp[e] + bv[e]);
        if (*(volatile LAS unsigned*)(L + L_FLAG + FL_ABORT * 4)) r = f32x4{NAN, NAN, NAN, NAN};
        *(GAS v2u*)(p.out + (lane >> 1) * D + 8 * a + 4 * (lane & 1)) = v2u{pack2(r[0], r[1]), pack2(r[2], r[3])};
        if (a == 0 && lane == 0) __hip_atomic_store((GAS unsigned*)(p.ws + WS_EPOCH), tg.a, __ATOMIC_RELAXED, __HIP_MEMORY_SCOPE_AGENT);
      }
    }
  }
  SSTAMP(11);
  SSTAMP_FLUSH();
}

__device__ __forceinline__ void ln_row(f32x4 (&v)[4]) {
  const float shift = __builtin_bit_cast(float, __builtin_amdgcn_readfirstlane(__builtin_bit_cast(int, v[0][0])));
  float s = 0.f, q = 0.f;
#pragma unroll
  for (int k = 0; k < 4; ++k) {
    v[k] -= shift;
    s += (v[k][0] + v[k][1]) + (v[k][2] + v[k][3]);
    q += (v[k][0] * v[k][0] + v[k][1] * v[k][1]) + (v[k][2] * v[k][2] + v[k][3] * v[k][3]);
  }
  wave_sum2(s, q);
  const float mean = s * (1.0f / D);
  const float var = fmaxf(q * (1.0f / D) - mean * mean, 0.f);
  const float r = __builtin_amdgcn_rsqf(var + LN_EPS);
#pragma unroll
  for (int k = 0; k < 4; ++k) v[k] = (v[k] - mean) * r;
}
__device__ __forceinline__ void chain_b(const Params& p, lptr L, __amdgpu_buffer_rsrc_t rs, int x, int i, int cw, int lane) {
  SSTAMP_DECL;
  const int j = 16 * x + (i - 16), ct = cw * 64 + lane, g = lane >> 4;
  const int ks = x & 3, cgrp = (x >> 2) * 16 + (i - 16);
  const int b = cw >> 1, gi = 32 * (cw & 1) + (lane & 31);
  v4u W[16];
  v4u xa = *(const GAS v4u*)(p.x + b * D + 16 * lane), xb = *(const GAS v4u*)(p.x + b * D + 16 * lane + 8);
  unsigned xs = *(const GAS unsigned*)(p.x + b * D + 256 * ks + 4 * gi + 2 * (lane >> 5));
  Tag tg = {__hip_atomic_load((GAS unsigned*)(p.ws + WS_EPOCH), __ATOMIC_RELAXED, __HIP_MEMORY_SCOPE_AGENT), 0u};
  __builtin_amdgcn_sched_barrier(0);
  if (WQ_DELAY) __builtin_amdgcn_s_sleep(WQ_DELAY);
  load_tile128(W, p.wq, 4096, 256 * ks, 128 * cgrp, cw, lane);
  SSTAMP(12);
  unsigned b1u = *(const GAS u16*)(p.b1 + 32 * (j < 112 ? j : 0) + (lane & 31));
  __builtin_amdgcn_sched_barrier(0);
  const Delta dl = make_delta(lane);
  PIN(xa); PIN(xb); PIN(xs); PIN(tg.a);
  SSTAMP(13);
  tg.a += 1u; tg.b = ~tg.a;
  {
    f32x4 xv[4];
#pragma unroll
    for (int e = 0; e < 2; ++e) {
      xv[e] = f32x4{bf_lo(xa[2 * e]), bf_hi(xa[2 * e]), bf_lo(xa[2 * e + 1]), bf_hi(xa[2 * e + 1])};
      xv[2 + e] = f32x4{bf_lo(xb[2 * e]), bf_hi(xb[2 * e]), bf_lo(xb[2 * e + 1]), bf_hi(xb[2 * e + 1])};
    }
    const float shift = __builtin_bit_cast(float, __builtin_amdgcn_readfirstlane(__builtin_bit_cast(int, xv[0][0])));
    float s = 0.f, q = 0.f;
#pragma unroll
    for (int k = 0; k < 4; ++k)
#pragma unroll
      for (int e = 0; e < 4; ++e) { const float d = xv[k][e] - shift; s += d; q += d * d; }
    wave_sum2(s, q);
    const float mean = s * (1.0f / D);
    const float rstd = __builtin_amdgcn_rsqf(fmaxf(q * (1.0f / D) - mean * mean, 0.f) + LN_EPS);
    float xn0 = (bf_lo(xs) - shift - mean) * rstd, xn1 = (bf_hi(xs) - shift - mean) * rstd;
    PIN(xn0); PIN(xn1);
    int off[4]; v4u v[4];
#pragma unroll
    for (int k = 0; k < 4; ++k) { off[k] = WS_F1 + ((k * 2 + b) * 512 + (lane >> 5) * 256 + 64 * ks + gi) * 16; PIN(off[k]); }
    int ao = L_ACT + (((b * 4 + (gi >> 4)) * 4 + 2 * (lane >> 5)) * 16 + (gi & 15)) * 2;
    PIN(ao);
    sweep<4, CAN_FRONT>(L, rs, off, v, tg);
    SSTAMP(15);
    const f32x4 t = (gval(v[0]) + gval(v[1])) + (gval(v[2]) + gval(v[3]));
    const unsigned lo = pack2(t[0], t[1]), hi = pack2(t[2], t[3]);
    const auto slo = __builtin_amdgcn_permlane32_swap(lo, lo, false, false), shi = __builtin_amdgcn_permlane32_swap(hi, hi, false, false);
    const unsigned sa = lane < 32 ? slo[0] : shi[0], ca = lane < 32 ? slo[1] : shi[1];
    const unsigned hp = pack2(xn0 * (1.0f + bf_lo(ca)) + bf_lo(sa), xn1 * (1.0f + bf_hi(ca)) + bf_hi(sa));
    lds_st<u16>(L + ao, (u16)hp);
    lds_st<u16>(L + ao + 32, (u16)(hp >> 16));
  }
  int offq[4];
  q_offsets(offq, x, ct);
  int f2off = WS_F2 + ((ks * 2 + (g >> 1)) * 1024 + 32 * cgrp + 2 * (lane & 15) + (g & 1)) * 16;
  PIN(f2off);
  wg_barrier();
  SSTAMP(1);
  {
    f32x4 s = gemv_tile128(L + L_ACT, W, dl, cw, lane);
    if (cw != 0) lds_st<f32x4>(L + L_RED + cw * 1024 + lane * 16, s);
    wg_barrier();
    if (cw == 0) {
#pragma unroll
      for (int ww = 1; ww < 4; ++ww) s += lds_ld<f32x4>(L + L_RED + ww * 1024 + lane * 16);
      put4(rs, f2off, s, tg);
    }
  }
  const unsigned long long tq = now10ns();
  SSTAMP(2);
  delay_since(tq, DLY_QB);
  gather_q<CAN_QB>(L, rs, offq, ct, tg);
  PIN(b1u);
  SSTAMP(3);
  if (cw == 0) stats_exchange(L, rs, x, i, lane, tg);
  if (W2EARLY && j < 112) {
    const int c = lane & 15;
    flag_wait<8>(L, FL_STAT, W2EARLY);
#pragma unroll
    for (int u = 0; u < 4; ++u) {
      if (u >= 1) WAIT_VM(0);
#pragma unroll
      for (int v = 0; v < 4; ++v) { const int e = 4 * u + v; W[e] = ldw(p.w2 + (size_t)(32 * j + 4 * (e & 7) + g) * D + (2 * cw + (e >> 3)) * 128 + 8 * c); }
      __builtin_amdgcn_sched_barrier(0);
    }
  }
  SSTAMP(5);
  int off3[4];
  if (j < 112) {
    int off[4]; v4u v[4];
#pragma unroll
    for (int k = 0; k < 4; ++k) off[k] = WS_F3B + (k * 256 + ct) * 16;
    sweep<4, CAN_SLOW>(L, rs, off, v, tg);
#pragma unroll
    for (int k = 0; k < 4; ++k) lds_st<v2u>(L + L_SBCB + (k * 1024 + 4 * ct) * 2, v2u{v[k].x, v[k].z});
#pragma unroll
    for (int k = 0; k < 4; ++k) { off3[k] = WS_H3 + ((x * 128 + (k >> 1) * 64 + lane) * 8 + 2 * (cw & 1) + (k & 1)) * 16; PIN(off3[k]); }
  }
  unsigned voW[4];
#pragma unroll
  for (int v = 0; v < 4; ++v) { voW[v] = (16 * v + (lane >> 2)) * (FF * 2) + (((lane & 3) ^ (((lane >> 4) & 1) << 1)) << 4) - v * 1024; PIN(voW[v]); }
  SSTAMP(6);
  publish_partial(L, rs, x, i, ct, tg);
  SSTAMP(7);
  if (j >= 112) { SSTAMP_FLUSH(); return; }
  {
    const char* W1b = (const char*)p.w1 + (size_t)(256 * cw) * (FF * 2) + j * 64;
    const unsigned ring = (unsigned)(size_t)L + L_RING + cw * 32768;
#pragma unroll
    for (int u = 0; u < 4; ++u) {
      const char* b_ = W1b + (size_t)u * 64 * (FF * 2); const unsigned la_ = ring + u * 4096;
      if (u >= PACE) WAIT_VM(4 * (PACE - 1));
      if (!NOW12) DMA_TILE(voW[0], voW[1], voW[2], voW[3], b_, la_);
    }
    const int c = lane & 15;
#pragma unroll
    for (int u = 0; u < (W2EARLY || NOW12 ? 0 : 4); ++u) {
      if (4 + u >= PACE) WAIT_VM(4 * (PACE - 1));
#pragma unroll
      for (int v = 0; v < 4; ++v) { const int e = 4 * u + v; W[e] = ldw(p.w2 + (size_t)(32 * j + 4 * (e & 7) + g) * D + (2 * cw + (e >> 3)) * 128 + 8 * c); }
      __builtin_amdgcn_sched_barrier(0);
    }
  }
  WAIT_LGKM0();
  __builtin_amdgcn_s_barrier();
  WAIT_VM(W2EARLY || NOW12 ? 0 : 16);
  v4u w1f[8][2];
  {
    const int q = (lane & 15) >> 2, pp = lane & 3;
#pragma unroll
    for (int kk = 0; kk < 8; ++kk)
#pragma unroll
      for (int ch = 0; ch < 2; ++ch) {
        v2u w[2];
#pragma unroll
        for (int hh = 0; hh < 2; ++hh) {
          const int row = (kk & 1) * 32 + 16 * hh + 4 * g + q;
          const int pc = (2 * ch + (pp >> 1)) ^ (((row >> 2) & 1) << 1);
          w[hh] = lds_tr(L + L_RING + cw * 32768 + (kk >> 1) * 4096 + row * 64 + pc * 16 + (pp & 1) * 8);
        }
        w1f[kk][ch] = v4u{w[0].x, w[0].y, w[1].x, w[1].y};
        PIN(w1f[kk][ch]);
      }
  }
  if (cw < 2 * LNPOLL) {
    const int cwb = cw & 1;
    f32x4 sb[4], cb[4];
#pragma unroll
    for (int k = 0; k < 4; ++k) {
      const int col = 8 * ((k >> 1) * 64 + lane) + 4 * (k & 1);
      const v2u s = lds_ld<v2u>(L + L_SBCB + ((cwb * 2 + 0) * 1024 + col) * 2), c = lds_ld<v2u>(L + L_SBCB + ((cwb * 2 + 1) * 1024 + col) * 2);
      sb[k] = f32x4{bf_lo(s.x), bf_hi(s.x), bf_lo(s.y), bf_hi(s.y)};
      cb[k] = f32x4{1.0f + bf_lo(c.x), 1.0f + bf_hi(c.x), 1.0f + bf_lo(c.y), 1.0f + bf_hi(c.y)};
      PIN(sb[k]); PIN(cb[k]);
    }
    v4u v[4];
    if (cw >= 2) __builtin_amdgcn_s_sleep(LNSTAG);
    sweep<4, CAN_H3>(L, rs, off3, v, tg);
    SSTAMP(8);
    if (LNPOLL == 1 || flag_claim(L, FL_LN0 + cwb)) {
      f32x4 y[4];
#pragma unroll
      for (int k = 0; k < 4; ++k) y[k] = gval(v[k]);
      ln_row(y);
#pragma unroll
      for (int k = 0; k < 4; ++k) y[k] = y[k] * cb[k] + sb[k];
      ln_row(y);
#pragma unroll
      for (int k = 0; k < 4; ++k) {
        const int gq = 2 * ((k >> 1) * 64 + lane) + (k & 1);
        const int pos = (gq >> 3) * 32 + (gq & 3) * 8 + ((gq >> 2) & 1) * 4;
        lds_st<v2u>(L + L_Y + (cwb * D + pos) * 2, v2u{pack2(y[k][0], y[k][1]), pack2(y[k][2], y[k][3])});
      }
    }
  }
  WAIT_LGKM0();
  __builtin_amdgcn_s_barrier();
  SSTAMP(9);
  {
    f32x4 acc[2] = {{0.f, 0.f, 0.f, 0.f}, {0.f, 0.f, 0.f, 0.f}};
#pragma unroll
    for (int kk = 0; kk < 8; ++kk) {
      const v4u ya = lds_ld<v4u>(L + L_Y + ((lane & 1) * D + 256 * cw + 32 * kk + 8 * g) * 2);
      acc[0] = mfma32(ya, w1f[kk][0], acc[0]);
      acc[1] = mfma32(ya, w1f[kk][1], acc[1]);
    }
    if (lane < 16) {
#pragma unroll
      for (int ch = 0; ch < 2; ++ch)
#pragma unroll
        for (int bb = 0; bb < 2; ++bb) lds_st<float>(L + L_RED + (cw * 64 + bb * 32 + ch * 16 + lane) * 4, acc[ch][bb]);
    }
    WAIT_LGKM0();
    __builtin_amdgcn_s_barrier();
    float a = bf_lo(b1u);
#pragma unroll
    for (int ww = 0; ww < 4; ++ww) a += lds_ld<float>(L + L_RED + (ww * 64 + lane) * 4);
    const float hv = gelu_tanh(rbf(a));
    const int k = lane & 31;
    lds_st<u16>(L + L_HID + cw * 128 + ((lane >> 5) * 32 + (k & 3) * 8 + (k >> 2)) * 2, (u16)pack2(hv, hv));
    const v4u av = lds_ld<v4u>(L + L_HID + cw * 128 + (((lane >> 3) & 1) * 32 + g * 8) * 2);
    f32x4 acc0, acc1;
    gemv_rows32(W, dl, av, acc0, acc1);
    put_owner(rs, WS_H4, j, cw, lane, acc0, acc1, tg);
  }
  SSTAMP(10);
  SSTAMP_FLUSH();
}

__global__ void __launch_bounds__(512, 2) fused_decode_block(Params p) {
  extern __shared__ __attribute__((aligned(16))) unsigned char smem[];
  lptr L = (lptr)smem;
  const int tid = threadIdx.x, lane = tid & 63, wave = __builtin_amdgcn_readfirstlane(tid >> 6);
  const int x = blockIdx.x & 7, i = blockIdx.x >> 3;
  if (wave < 4) {
    if (tid < 16) { *(LAS unsigned*)(L + L_FLAG + tid * 4) = 0u; WAIT_LGKM0(); }
    if (i < 16) stream_wave<NT_A, 8, PW_A, LA_A, 1>(p, L, x, i, wave, lane);
    else stream_wave<32 - NT_A, LA_B, LA_B, LA_B, 3>(p, L, x, i, wave, lane);
    return;
  }
  __builtin_amdgcn_s_setprio(2);
  const __amdgpu_buffer_rsrc_t rs = __builtin_amdgcn_make_buffer_rsrc((void*)p.ws, 0, WS_END, 0x00020000);
  if (i < 16) chain_a(p, L, rs, x, i, wave - 4, lane);
  else chain_b(p, L, rs, x, i, wave - 4, lane);
}

}

extern "C" void kernel_launch(void* const* d_in, const int* in_sizes, int n_in, void* d_out, int out_size, void* d_ws, size_t ws_size,
                              hipStream_t stream) {
  Params p;
  p.x = (const u16*)d_in[0];
  p.K = (const u16*)d_in[1];
  p.V = (const u16*)d_in[2];
  p.wq = (const u16*)d_in[3];
  p.wo = (const u16*)d_in[4];
  p.w1 = (const u16*)d_in[6];
  p.b1 = (const u16*)d_in[7];
  p.w2 = (const u16*)d_in[8];
  p.b2 = (const u16*)d_in[9];
  p.c = (const u16*)d_in[10];
  p.wmod = (const u16*)d_in[11];
  p.out = (u16*)d_out;
  p.ws = (unsigned char*)d_ws;
  (void)hipFuncSetAttribute((const void*)fused_decode_block, hipFuncAttributeMaxDynamicSharedMemorySize, LDS_BYTES);
  hipLaunchKernelGGL(fused_decode_block, dim3(256), dim3(512), LDS_BYTES, stream, p);
}
```

```cpp
#define CAN_H1 1
#define CAN_H2 1
#define DLY_H2 0
#include <hip/hip_runtime.h>
#include <hip/hip_bf16.h>
#include <stdint.h>

#define GAS __attribute__((address_space(1)))
#define LAS __attribute__((address_space(3)))
typedef unsigned v4u __attribute__((ext_vector_type(4)));
typedef unsigned v2u __attribute__((ext_vector_type(2)));
typedef float f32x4 __attribute__((ext_vector_type(4)));
typedef __bf16 bf16x8 __attribute__((ext_vector_type(8)));
typedef __bf16 bf16x2 __attribute__((ext_vector_type(2)));
typedef short s16x4 __attribute__((ext_vector_type(4)));
typedef unsigned short u16;
typedef LAS unsigned char* lptr;

#define STAMPS 0
#define PW_A 4
#define LA_A 3
#define LA_B 4
#define LA_SM 6
#define NT_A 17
#define PRE_B 2
#define PACE 2
#define CAN_QA 0
#define CAN_QB 0
#define DLY_QB 50
#define W2EARLY 0
#define NOW12 0
#define NPOLL 1
#define STAG 9
#define LNPOLL 1
#define LNSTAG 6
#define W2FRONT 0
#define WO_LATE 0
#define WO_BACK 4
#define W12_DELAY 0
#define ROLEMAP 1
#define PRE_B_SLEEP 50
#define LA_B_EXTRA 0
#define MOD_LATE 0
#define WQ_DELAY 12
#define CAN_FRONT 1
#define CAN_SLOW 16
#define CAN_H3 1
#define DLY_H4 150
#define CAN_H4 1

namespace {

constexpr int D = 1024, SK = 32768, FF = 3584, NMOD = 6144;
constexpr float SC_LOG2 = 0.08838834764831845f * 1.4426950408889634f;
constexpr float LN_EPS = 1e-5f;

constexpr int WS_EPOCH = 0;
constexpr int WS_F1  = 4096;
constexpr int WS_F2  = WS_F1 + 4096 * 16;
constexpr int WS_F3A = WS_F2 + 8192 * 16;
constexpr int WS_F3B = WS_F3A + 8192 * 16;
constexpr int WS_ST  = WS_F3B + 1024 * 16;
constexpr int WS_H1  = WS_ST + 2048 * 16;
constexpr int WS_H2  = WS_H1 + 65536 * 16;
constexpr int NREP3  = 8;
constexpr int WS_H3  = WS_H2 + 65536 * 16;
constexpr int WS_H4  = WS_H3 + NREP3 * 1024 * 16;
constexpr int WS_END = WS_H4 + 65536 * 16;
constexpr int WS_STAMP = 8 << 20;

constexpr int L_RING = 0;
constexpr int L_MISC = 131072;
constexpr int L_ACT  = L_MISC;
constexpr int L_Q    = L_ACT + 1024;
constexpr int L_RED  = L_Q + 2048;
constexpr int L_SBCB = L_RED + 4096;
constexpr int L_Y    = L_SBCB + 8192;
constexpr int L_STAT = L_Y + 4096;
constexpr int L_FAC  = L_STAT + 256;
constexpr int L_GAB  = L_FAC + 128;
constexpr int L_HID  = L_GAB + 128;
constexpr int L_RED2 = L_HID + 512;
constexpr int L_FLAG = L_RED2 + 1024;
constexpr int LDS_BYTES = 163840;
static_assert(L_FLAG + 64 <= LDS_BYTES, "LDS map");
enum { FL_SYNC = 0, FL_Q = 1, FL_STAT = 2, FL_FAC = 3, FL_ODONE = 4, FL_LASTV = 5, FL_ABORT = 6, FL_GO = 7, FL_RED = 8, FL_GO0 = 9, FL_X1 = 10, FL_OUT = 11, FL_LN0 = 12, FL_LN1 = 13 };

constexpr unsigned SPIN_GLOBAL = 1u << 16;
constexpr unsigned SPIN_LDS = 1u << 21;

struct Params {
  const u16 *x, *K, *V, *wq, *wo, *w1, *b1, *w2, *b2, *c, *wmod;
  u16* out;
  unsigned char* ws;
};

#define PIN(x) asm volatile("" : "+v"(x))
__device__ __forceinline__ float bf_lo(unsigned u) { return __builtin_bit_cast(float, u << 16); }
__device__ __forceinline__ float bf_hi(unsigned u) { return __builtin_bit_cast(float, u & 0xffff0000u); }
__device__ __forceinline__ unsigned pack2(float lo, float hi) { bf16x2 r = {(__bf16)lo, (__bf16)hi}; return __builtin_bit_cast(unsigned, r); }
__device__ __forceinline__ float rbf(float v) { return bf_lo(pack2(v, v)); }
template <int CTRL> __device__ __forceinline__ float dpp(float v) {
  return __builtin_bit_cast(float, __builtin_amdgcn_mov_dpp(__builtin_bit_cast(int, v), CTRL, 0xf, 0xf, true));
}
constexpr int XOR1 = 0xB1, XOR2 = 0x4E, ROR4 = 0x124, ROR8 = 0x128;
__device__ __forceinline__ float sum_16_32(float v) {
  auto s = __builtin_amdgcn_permlane16_swap(__float_as_uint(v), __float_as_uint(v), false, false);
  v = __uint_as_float(s[0]) + __uint_as_float(s[1]);
  auto t = __builtin_amdgcn_permlane32_swap(__float_as_uint(v), __float_as_uint(v), false, false);
  return __uint_as_float(t[0]) + __uint_as_float(t[1]);
}
__device__ __forceinline__ float max_16_32(float v) {
  auto s = __builtin_amdgcn_permlane16_swap(__float_as_uint(v), __float_as_uint(v), false, false);
  v = fmaxf(__uint_as_float(s[0]), __uint_as_float(s[1]));
  auto t = __builtin_amdgcn_permlane32_swap(__float_as_uint(v), __float_as_uint(v), false, false);
  return fmaxf(__uint_as_float(t[0]), __uint_as_float(t[1]));
}
__device__ __forceinline__ void wave_sum2(float& a, float& b) {
  a += dpp<XOR1>(a); b += dpp<XOR1>(b);
  a += dpp<XOR2>(a); b += dpp<XOR2>(b);
  a += dpp<ROR4>(a); b += dpp<ROR4>(b);
  a += dpp<ROR8>(a); b += dpp<ROR8>(b);
  a = sum_16_32(a); b = sum_16_32(b);
}
__device__ __forceinline__ f32x4 mfma32(v4u a, v4u b, f32x4 c) {
  return __builtin_amdgcn_mfma_f32_16x16x32_bf16(__builtin_bit_cast(bf16x8, a), __builtin_bit_cast(bf16x8, b), c, 0, 0, 0);
}
__device__ __forceinline__ f32x4 mfma16(v2u a, v2u b, f32x4 c) {
  return __builtin_amdgcn_mfma_f32_16x16x16bf16_1k(__builtin_bit_cast(s16x4, a), __builtin_bit_cast(s16x4, b), c, 0, 0, 0);
}
__device__ __forceinline__ v2u lds_tr(lptr p) {
  return __builtin_bit_cast(v2u, __builtin_amdgcn_ds_read_tr16_b64_v4i16((LAS s16x4*)p));
}
template <class T> __device__ __forceinline__ T lds_ld(lptr p) { return *(LAS T*)p; }
template <class T> __device__ __forceinline__ void lds_st(lptr p, T v) { *(LAS T*)p = v; }
__device__ __forceinline__ v4u ldw(const u16* p) { return __builtin_nontemporal_load((const GAS v4u*)p); }
#define WAIT_VM(n) asm volatile("s_waitcnt vmcnt(%0)" ::"i"(n) : "memory")
#define WAIT_LGKM0() asm volatile("s_waitcnt lgkmcnt(0)" ::: "memory")
__device__ __forceinline__ unsigned long long now10ns() { return __builtin_amdgcn_s_memrealtime(); }

#define SSTAMP(idx)
#define SSTAMP_DECL
#define TPARAM
#define SSTAMP_FLUSH()

__device__ __forceinline__ void flag_add(lptr L, int f) {
  unsigned addr = (unsigned)(size_t)L + L_FLAG + f * 4;
  unsigned one = 1u;
  unsigned long long keep;
  asm volatile("s_waitcnt lgkmcnt(0)\n\ts_mov_b64 %0, exec\n\ts_mov_b64 exec, 1\n\tds_add_u32 %1, %2\n\ts_mov_b64 exec, %0"
               : "=&s"(keep) : "v"(addr), "v"(one) : "memory");
}
__device__ __forceinline__ bool flag_claim(lptr L, int f) {
  unsigned old = 0;
  if ((threadIdx.x & 63) == 0) old = __hip_atomic_fetch_add((LAS unsigned*)(L + L_FLAG) + f, 1u, __ATOMIC_RELAXED, __HIP_MEMORY_SCOPE_WORKGROUP);
  return __builtin_amdgcn_readfirstlane(old) == 0u;
}
__device__ __forceinline__ void wg_barrier() { WAIT_LGKM0(); __builtin_amdgcn_s_barrier(); asm volatile("" ::: "memory"); }
template <int SLEEP>
__device__ __forceinline__ void flag_wait(lptr L, int f, unsigned want) {
  volatile LAS unsigned* fl = (volatile LAS unsigned*)(L + L_FLAG);
#pragma nounroll
  for (unsigned sp = 0; fl[f] < want; ++sp) {
    if (sp > SPIN_LDS) { fl[FL_ABORT] = 1; break; }
    if (SLEEP) __builtin_amdgcn_s_sleep(SLEEP);
  }
  asm volatile("" ::: "memory");
}

struct Tag { unsigned a, b; };
__device__ __forceinline__ void put(__amdgpu_buffer_rsrc_t rs, int off, unsigned lo, unsigned hi, Tag t) {
  v4u g = {lo, t.a, hi, t.b};
  __builtin_amdgcn_raw_buffer_store_b128(g, rs, off, 0, 16);
}
__device__ __forceinline__ void put4(__amdgpu_buffer_rsrc_t rs, int off, f32x4 v, Tag t) { put(rs, off, pack2(v[0], v[1]), pack2(v[2], v[3]), t); }
template <int NL, int CAN>
__device__ __forceinline__ void sweep(lptr L, __amdgpu_buffer_rsrc_t rs, const int (&off)[NL], v4u (&v)[NL], Tag t) {
  if (CAN > 0) {
    const int oc = __builtin_amdgcn_readlane(off[NL - 1], 63);
#pragma nounroll
    for (unsigned sp = 0;; ++sp) {
      const v4u c = __builtin_amdgcn_raw_buffer_load_b128(rs, 0, oc, 16);
      if (__all((c.y == t.a) & (c.w == t.b))) break;
      if (sp > SPIN_GLOBAL) break;
      if (CAN > 1) __builtin_amdgcn_s_sleep(CAN - 1);
      asm volatile("" ::: "memory");
    }
  }
#pragma nounroll
  for (unsigned sp = 0;; ++sp) {
#pragma unroll
    for (int k = 0; k < NL; ++k) v[k] = __builtin_amdgcn_raw_buffer_load_b128(rs, off[k], 0, 16);
    bool ok = true;
#pragma unroll
    for (int k = 0; k < NL; ++k) ok &= (v[k].y == t.a) & (v[k].w == t.b);
    if (__all(ok)) break;
    if (sp > SPIN_GLOBAL) { *(volatile LAS unsigned*)(L + L_FLAG + FL_ABORT * 4) = 1; break; }
    asm volatile("" ::: "memory");
  }
}
__device__ __forceinline__ void delay_since(unsigned long long t0, int ticks) {
  if (ticks > 0) { while (now10ns() - t0 < (unsigned long long)ticks) {} }
}
__device__ __forceinline__ f32x4 gval(v4u g) { return f32x4{bf_lo(g.x), bf_hi(g.x), bf_lo(g.z), bf_hi(g.z)}; }
__device__ __forceinline__ v2u sel_op(int lane) {
  const int m = lane & 3;
  return v2u{m == 0 ? 0x00003f80u : m == 1 ? 0x3f800000u : 0u, m == 2 ? 0x00003f80u : m == 3 ? 0x3f800000u : 0u};
}
__device__ __forceinline__ f32x4 gsum(v2u sel, v4u g, f32x4 acc) { return mfma16(sel, v2u{g.x, g.z}, acc); }
__device__ __forceinline__ f32x4 gsum_t(v2u sel, v4u g, f32x4 acc) { return mfma16(v2u{g.x, g.z}, sel, acc); }

struct Delta { unsigned m[4]; };
__device__ __forceinline__ Delta make_delta(int lane) {
  const int j = lane & 7;
  Delta d;
#pragma unroll
  for (int r = 0; r < 4; ++r) { d.m[r] = (r == (j >> 1)) ? ((j & 1) ? 0xffff0000u : 0x0000ffffu) : 0u; PIN(d.m[r]); }
  return d;
}
__device__ __forceinline__ v4u delta_op(const Delta& d, unsigned pair, int hi) {
  const unsigned dup = __builtin_amdgcn_perm(pair, pair, hi ? 0x03020302u : 0x01000100u);
  return v4u{dup & d.m[0], dup & d.m[1], dup & d.m[2], dup & d.m[3]};
}

#define DMA_TILE(o0, o1, o2, o3, base, ldsaddr)                                                          \
  do {                                                                                                   \
    unsigned keep_;                                                                                      \
    asm volatile("s_nop 4\n\ts_mov_b32 %0, m0\n\ts_mov_b32 m0, %6\n\ts_nop 0\n\t"                        \
                 "global_load_lds_dwordx4 %1, %5 nt\n\t"                                                 \
                 "global_load_lds_dwordx4 %2, %5 offset:1024 nt\n\t"                                     \
                 "global_load_lds_dwordx4 %3, %5 offset:2048 nt\n\t"                                     \
                 "global_load_lds_dwordx4 %4, %5 offset:3072 nt\n\t"                                     \
                 "s_mov_b32 m0, %0"                                                                      \
                 : "=&s"(keep_)                                                                          \
                 : "v"(o0), "v"(o1), "v"(o2), "v"(o3), "s"(base), "s"(ldsaddr)                           \
                 : "memory");                                                                            \
  } while (0)

constexpr int last_issued(int t, int npre, int la, int nt) {
  int li = npre - 1;
  for (int s = 0; s <= t; ++s) {
    int ahead = la;
    if (s >= nt - 3 && s <= nt - 1) ahead = LA_SM > la ? LA_SM : la;
    int want = s + ahead;
    if (npre > la) {
      const int w2 = s + 8 < npre - 1 + la ? s + 8 : npre - 1 + la;
      if (w2 > want) want = w2;
    }
    if (want > s + 8) want = s + 8;
    if (want > 2 * nt - 1) want = 2 * nt - 1;
    if (want > li) li = want;
  }
  return li;
}
template <int NT, int NPRE, int PW, int LA, int NBAR, int NEARLY>
__device__ __forceinline__ void stream_wave(const Params& p, lptr L, int x, int i, int w, int lane TPARAM) {
  SSTAMP_DECL;
  const int g = lane >> 4, n = lane & 15;
  const size_t key0 = (size_t)x * SK + (i < 16 ? 64 * NT_A * i : 1024 * NT_A + 64 * (32 - NT_A) * (i - 16)) + 16 * NT * w;
  const char* Kb = (const char*)p.K + key0 * 256;
  const char* Vb = (const char*)p.V + key0 * 256;
  const unsigned ring = (unsigned)(size_t)L + L_RING + w * 32768;
  unsigned voK[4], voV[2];
#pragma unroll
  for (int v = 0; v < 4; ++v) voK[v] = g * 256 + ((n ^ (4 * v + g)) << 4);
#pragma unroll
  for (int v = 0; v < 2; ++v) voV[v] = g * 256 + ((n ^ (((4 * v + g) & 7) << 1)) << 4);

#define ISSUE(t)                                                                                                \
  do {                                                                                                          \
    const unsigned la_ = ring + ((t)&7) * 4096;                                                                 \
    if ((t) < NT) { const char* b_ = Kb + (t)*4096; DMA_TILE(voK[0], voK[1], voK[2], voK[3], b_, la_); }         \
    else { const char* b_ = Vb + ((t)-NT) * 4096; DMA_TILE(voV[0], voV[1], voV[0], voV[1], b_, la_); }          \
  } while (0)
#define LI(t) last_issued(t, NPRE, LA, NT)
#define REFILL(t)                                                                       \
  do {                                                                                  \
    _Pragma("unroll") for (int u_ = LI((t)-1) + 1; u_ <= LI(t); ++u_) ISSUE(u_);          \
  } while (0)

  if (NEARLY > 0) {
    __builtin_amdgcn_s_sleep(PRE_B_SLEEP);
#pragma unroll
    for (int t = 0; t < NEARLY; ++t) ISSUE(t);
  }
#pragma unroll
  for (int k = 0; k < NBAR; ++k) __builtin_amdgcn_s_barrier();
  SSTAMP(7);
  if (NEARLY == 0) {
#pragma unroll
    for (int t = 0; t < PW; ++t) ISSUE(t);
#pragma unroll
    for (int t = PW; t < NPRE; ++t) { WAIT_VM(4 * (PW - 1)); ISSUE(t); }
  } else {
#pragma unroll
    for (int t = NEARLY; t < NPRE; ++t) ISSUE(t);
  }
  SSTAMP(1);
  if (NBAR == 1) __builtin_amdgcn_s_barrier();
  asm volatile("" ::: "memory");
  SSTAMP(2);

  v4u qf[4];
#pragma unroll
  for (int t4 = 0; t4 < 4; ++t4) qf[t4] = lds_ld<v4u>(L + L_Q + (n & 7) * 256 + t4 * 64 + g * 16);
  lptr rb = L + L_RING + w * 32768;
  int ka[4];
#pragma unroll
  for (int t4 = 0; t4 < 4; ++t4) ka[t4] = n * 256 + (((4 * t4 + g) ^ n) << 4);

  f32x4 sc[NT];
#pragma unroll
  for (int t = 0; t < NT; ++t) {
    WAIT_VM(4 * (LI(t - 1) - t));
    v4u kf[4];
#pragma unroll
    for (int t4 = 0; t4 < 4; ++t4) kf[t4] = lds_ld<v4u>(rb + (t & 7) * 4096 + ka[t4]);
    WAIT_LGKM0();
    REFILL(t);
    f32x4 a = {0.f, 0.f, 0.f, 0.f};
#pragma unroll
    for (int t4 = 0; t4 < 4; ++t4) a = mfma32(kf[t4], qf[t4], a);
    sc[t] = a;
  }
  SSTAMP(3);
  float m = -INFINITY;
#pragma unroll
  for (int t = 0; t < NT; ++t) m = fmaxf(fmaxf(fmaxf(sc[t][0], sc[t][1]), fmaxf(sc[t][2], sc[t][3])), m);
  m = max_16_32(m);
  const float mm = m * SC_LOG2;
  float lsum = 0.f;
  v2u pb[NT];
#pragma unroll
  for (int t = 0; t < NT; ++t) {
    float e[4];
#pragma unroll
    for (int r = 0; r < 4; ++r) { e[r] = __builtin_amdgcn_exp2f(fmaf(sc[t][r], SC_LOG2, -mm)); lsum += e[r]; }
    pb[t] = v2u{pack2(e[0], e[1]), pack2(e[2], e[3])};
  }
  lsum = sum_16_32(lsum);
  if (lane < 8) lds_st<v2u>(L + L_STAT + (w * 8 + lane) * 8, v2u{__float_as_uint(mm), __float_as_uint(lsum)});
  flag_add(L, FL_STAT);

  int va[8];
  {
    const int q = (lane & 15) >> 2, pp = lane & 3, row = 4 * g + q;
#pragma unroll
    for (int u = 0; u < 8; ++u) va[u] = row * 256 + (((2 * u + (pp >> 1)) ^ ((row & 7) << 1)) << 4) + (pp & 1) * 8;
  }
  f32x4 o[8];
#pragma unroll
  for (int u = 0; u < 8; ++u) o[u] = f32x4{0.f, 0.f, 0.f, 0.f};
#pragma unroll
  for (int t = NT; t < 2 * NT; ++t) {
    WAIT_VM(4 * (LI(t - 1) - t));
    v2u vf[8];
#pragma unroll
    for (int u = 0; u < 8; ++u) vf[u] = lds_tr(rb + (t & 7) * 4096 + va[u]);
    WAIT_LGKM0();
    REFILL(t);
    if (WO_LATE == 1 && NBAR == 1 && t == 2 * NT - WO_BACK) flag_add(L, FL_LASTV);
#pragma unroll
    for (int u = 0; u < 8; ++u) o[u] = mfma16(vf[u], pb[t - NT], o[u]);
  }
  SSTAMP(4);
  if (n < 8) {
#pragma unroll
    for (int u = 0; u < 8; ++u) lds_st<f32x4>(rb + 4 * 4096 + n * 528 + (16 * u + 4 * g) * 4, o[u]);
  }
  flag_add(L, FL_ODONE);
  SSTAMP(5);
  SSTAMP_FLUSH();
#undef ISSUE
#undef LI
#undef REFILL
}

__device__ __forceinline__ f32x4 gemv_tile128(lptr act, const v4u (&W)[16], const Delta& dl, int cw, int lane) {
  const int g = lane >> 4, b = (lane >> 3) & 1;
  const v4u a0 = lds_ld<v4u>(act + ((b * 4 + cw) * 4 + g) * 32), a1 = lds_ld<v4u>(act + ((b * 4 + cw) * 4 + g) * 32 + 16);
  f32x4 acc0 = {0.f, 0.f, 0.f, 0.f}, acc1 = acc0;
#pragma unroll
  for (int u = 0; u < 8; ++u) {
    acc0 = mfma32(delta_op(dl, a0[u >> 1], u & 1), W[u], acc0);
    acc1 = mfma32(delta_op(dl, a1[u >> 1], u & 1), W[8 + u], acc1);
  }
  return acc0 + acc1;
}
__device__ __forceinline__ f32x4 reduce_to_wave0(lptr L, int cw, int lane, f32x4 acc, unsigned want) {
  if (cw != 0) { lds_st<f32x4>(L + L_RED + cw * 1024 + lane * 16, acc); flag_add(L, FL_RED); return acc; }
  flag_wait<0>(L, FL_RED, want);
#pragma unroll
  for (int ww = 1; ww < 4; ++ww) acc += lds_ld<f32x4>(L + L_RED + ww * 1024 + lane * 16);
  return acc;
}
__device__ __forceinline__ void gemv_rows32(const v4u (&W)[16], const Delta& dl, v4u av, f32x4& acc0, f32x4& acc1) {
  acc0 = f32x4{0.f, 0.f, 0.f, 0.f}; acc1 = acc0;
#pragma unroll
  for (int u = 0; u < 8; ++u) {
    const v4u a = delta_op(dl, av[u >> 1], u & 1);
    acc0 = mfma32(a, W[u], acc0);
    acc1 = mfma32(a, W[8 + u], acc1);
  }
}
__device__ __forceinline__ void load_rows32(v4u (&W)[16], const u16* Wg, int row0, int cw, int lane) {
  const int g = lane >> 4, c = lane & 15;
#pragma unroll
  for (int cb = 0; cb < 2; ++cb)
#pragma unroll
    for (int u = 0; u < 8; ++u) W[cb * 8 + u] = ldw(Wg + (size_t)(row0 + 4 * u + g) * D + (2 * cw + cb) * 128 + 8 * c);
  __builtin_amdgcn_sched_barrier(0);
}
__device__ __forceinline__ void load_tile128(v4u (&W)[16], const u16* Wg, int ld, int row0, int col0, int cw, int lane) {
  const int g = lane >> 4, c = lane & 15;
#pragma unroll
  for (int u = 0; u < 16; ++u) W[u] = ldw(Wg + (size_t)(row0 + 64 * cw + 4 * u + g) * ld + col0 + 8 * c);
  __builtin_amdgcn_sched_barrier(0);
}
__device__ __forceinline__ void put_owner(__amdgpu_buffer_rsrc_t rs, int base, int prod, int cw, int lane, f32x4 acc0, f32x4 acc1, Tag tg) {
  const int o0 = base + prod * 8192 + (32 * cw + (lane & 15)) * 64 + (lane >> 4) * 16;
  put4(rs, o0, acc0, tg);
  put4(rs, o0 + 1024, acc1, tg);
}
template <int NP, int CAN>
__device__ __forceinline__ f32x4 gather_owner(lptr L, __amdgpu_buffer_rsrc_t rs, int off0, v2u sel, Tag tg) {
  constexpr int NL = NP / 16;
  int off[NL]; v4u v[NL];
#pragma unroll
  for (int k = 0; k < NL; ++k) off[k] = off0 + k * 16 * 8192;
  sweep<NL, CAN>(L, rs, off, v, tg);
  f32x4 s = {0.f, 0.f, 0.f, 0.f};
#pragma unroll
  for (int k = 0; k < NL; ++k) s = gsum(sel, v[k], s);
#pragma unroll
  for (int e = 0; e < 4; ++e) { float a = s[e]; a += dpp<ROR4>(a); s[e] = a + dpp<ROR8>(a); }
  return s;
}
__device__ __forceinline__ float silu(float v) { return v * __builtin_amdgcn_rcpf(1.0f + __expf(-v)); }
__device__ __forceinline__ float gelu_tanh(float a) {
  const float u = 0.7978845608028654f * (a + 0.044715f * a * a * a);
  const float e = __expf(2.0f * u);
  const float th = 1.0f - 2.0f * __builtin_amdgcn_rcpf(e + 1.0f);
  return 0.5f * a * (1.0f + th);
}

template <int CAN>
__device__ __forceinline__ void gather_q(lptr L, __amdgpu_buffer_rsrc_t rs, const int (&off)[4], int ct, Tag tg) {
  v4u v[4];
  sweep<4, CAN>(L, rs, off, v, tg);
  const f32x4 s = (gval(v[0]) + gval(v[1])) + (gval(v[2]) + gval(v[3]));
  lds_st<v2u>(L + L_Q + ct * 8, v2u{pack2(s[0], s[1]), pack2(s[2], s[3])});
  wg_barrier();
}
__device__ __forceinline__ void q_offsets(int (&off)[4], int x, int ct) {
#pragma unroll
  for (int k = 0; k < 4; ++k) { off[k] = WS_F2 + ((k * 2 + (x >> 2)) * 1024 + (x & 3) * 256 + ct) * 16; PIN(off[k]); }
}
__device__ __forceinline__ void stats_exchange(lptr L, __amdgpu_buffer_rsrc_t rs, int x, int i, int lane, Tag tg) {
  flag_wait<4>(L, FL_STAT, 4);
  const int h = lane & 7;
  float mw[4], lw[4];
#pragma unroll
  for (int w = 0; w < 4; ++w) { const v2u s = lds_ld<v2u>(L + L_STAT + (w * 8 + h) * 8); mw[w] = __uint_as_float(s.x); lw[w] = __uint_as_float(s.y); }
  const float mc = fmaxf(fmaxf(mw[0], mw[1]), fmaxf(mw[2], mw[3]));
  float lc = 0.f;
#pragma unroll
  for (int w = 0; w < 4; ++w) lc += lw[w] * __builtin_amdgcn_exp2f(mw[w] - mc);
  if (lane < 8) put(rs, WS_ST + ((x * 32 + i) * 8 + h) * 16, __float_as_uint(mc), __float_as_uint(lc), tg);
  int off[4]; v4u v[4];
#pragma unroll
  for (int k = 0; k < 4; ++k) off[k] = WS_ST + ((x * 32 + (lane >> 3) + 8 * k) * 8 + h) * 16;
  sweep<4, CAN_SLOW>(L, rs, off, v, tg);
  float M = -INFINITY;
#pragma unroll
  for (int k = 0; k < 4; ++k) M = fmaxf(M, __uint_as_float(v[k].x));
  M = fmaxf(M, dpp<ROR8>(M));
  M = max_16_32(M);
  float S = 0.f;
#pragma unroll
  for (int k = 0; k < 4; ++k) S += __uint_as_float(v[k].z) * __builtin_amdgcn_exp2f(__uint_as_float(v[k].x) - M);
  S += dpp<ROR8>(S);
  S = sum_16_32(S);
  const float inv = 1.0f / S;
  if (lane < 32) {
    const int w = lane >> 3;
    const float mws = w == 0 ? mw[0] : w == 1 ? mw[1] : w == 2 ? mw[2] : mw[3];
    lds_st<float>(L + L_FAC + lane * 4, __builtin_amdgcn_exp2f(mws - M) * inv);
  }
  flag_add(L, FL_FAC);
}
__device__ __forceinline__ void publish_partial(lptr L, __amdgpu_buffer_rsrc_t rs, int x, int i, int ct, Tag tg) {
  const int h = ct >> 5, gi = ct & 31;
  int off = WS_H1 + (((((x >> 2) * 32 + (x & 3) * 8 + h) * 4 + (gi >> 3)) * 32 + i) * 8 + (gi & 7)) * 16;
  PIN(off);
  flag_wait<2>(L, FL_FAC, 1);
  float f[4];
#pragma unroll
  for (int w = 0; w < 4; ++w) { f[w] = lds_ld<float>(L + L_FAC + (w * 8 + h) * 4); PIN(f[w]); }
  flag_wait<0>(L, FL_ODONE, 4);
  f32x4 s = lds_ld<f32x4>(L + L_RING + 4 * 4096 + h * 528 + gi * 16) * f[0];
#pragma unroll
  for (int w = 1; w < 4; ++w) s += lds_ld<f32x4>(L + L_RING + w * 32768 + 4 * 4096 + h * 528 + gi * 16) * f[w];
  put4(rs, off, s, tg);
}

__device__ __forceinline__ void chain_a(const Params& p, lptr L, __amdgpu_buffer_rsrc_t rs, int x, int i, int cw, int lane TPARAM) {
  SSTAMP_DECL;
  const int a = 16 * x + i, ct = cw * 64 + lane, g = lane >> 4;
  const int cg = a >> 2, ks = a & 3, row0 = 256 * ks + 64 * cw;
  v4u W[16];
  unsigned cpair = *(const GAS unsigned*)(p.c + (lane >> 5) * D + row0 + 2 * (lane & 31));
  __builtin_amdgcn_sched_barrier(0);
#pragma unroll
  for (int u = 0; u < 8; ++u) W[u] = ldw(p.wmod + (size_t)(row0 + 8 * u + (lane >> 3)) * NMOD + 64 * cg + 8 * (lane & 7));
  __builtin_amdgcn_sched_barrier(0);
  Tag tg = {__hip_atomic_load((GAS unsigned*)(p.ws + WS_EPOCH), __ATOMIC_RELAXED, __HIP_MEMORY_SCOPE_AGENT), 0u};
  __builtin_amdgcn_sched_barrier(0);
  SSTAMP(12);
  const Delta dl = make_delta(lane);
  unsigned spair = pack2(silu(bf_lo(cpair)), silu(bf_hi(cpair)));
  PIN(spair);
  {
    int foff = WS_F1 + ((ks * 2 + (g >> 1)) * 512 + 16 * cg + 2 * (lane & 7) + (g & 1)) * 16;
    PIN(foff);
    const int rr = 2 * (lane & 31), b = lane >> 5;
    lds_st<unsigned>(L + L_ACT + cw * 256 + (b * 64 + ((rr >> 1) & 3) * 16 + (rr >> 3) * 2) * 2, spair);
    const int bb = (lane >> 3) & 1;
    const v4u a0 = lds_ld<v4u>(L + L_ACT + cw * 256 + (bb * 64 + g * 16) * 2), a1 = lds_ld<v4u>(L + L_ACT + cw * 256 + (bb * 64 + g * 16) * 2 + 16);
    v4u da[8][2];
#pragma unroll
    for (int u = 0; u < 8; ++u) {
      const unsigned pr = u < 4 ? a0[u] : a1[u - 4];
      da[u][0] = delta_op(dl, pr, 0); da[u][1] = delta_op(dl, pr, 1);
      PIN(da[u][0]); PIN(da[u][1]);
    }
    SSTAMP(13);
    f32x4 acc0 = {0.f, 0.f, 0.f, 0.f}, acc1 = acc0;
#pragma unroll
    for (int u = 0; u < 8; ++u) {
      acc0 = mfma32(da[u][0], W[u], acc0);
      acc1 = mfma32(da[u][1], W[u], acc1);
    }
    f32x4 t = (lane & 8) ? acc1 : acc0;
#pragma unroll
    for (int e = 0; e < 4; ++e) t[e] += dpp<ROR8>(t[e]);
    if (cw != 0) lds_st<f32x4>(L + L_RED + cw * 1024 + lane * 16, t);
    SSTAMP(14);
    wg_barrier();
    PIN(tg.a);
    tg.a += 1u; tg.b = ~tg.a;
    if (cw == 0) {
#pragma unroll
      for (int ww = 1; ww < 4; ++ww) t += lds_ld<f32x4>(L + L_RED + ww * 1024 + lane * 16);
      if ((lane & 8) == 0) put4(rs, foff, t, tg);
    }
  }
  SSTAMP(1);
  v2u xown = *(const GAS v2u*)(p.x + ((lane >> 1) & 1) * D + 8 * a + 4 * (lane & 1));
  v2u b2own = *(const GAS v2u*)(p.b2 + 8 * a + 4 * (lane & 1));
  __builtin_amdgcn_sched_barrier(0);
  v4u W2[16];
  if (!MOD_LATE) load_tile128(W2, p.wmod, NMOD, 256 * ks, 2048 + 128 * cg, cw, lane);
  if (!WO_LATE) load_rows32(W, p.wo, 32 * a, cw, lane);
  PIN(xown); PIN(b2own);
  {
    const int rr = 2 * (lane & 31), bb = lane >> 5;
    lptr ap = L + L_Y + (((bb * 4 + cw) * 4 + (rr & 3)) * 16 + (rr >> 2)) * 2;
    lds_st<u16>(ap, (u16)spair);
    lds_st<u16>(ap + 32, (u16)(spair >> 16));
  }
  { int offq[4]; q_offsets(offq, x, ct); gather_q<CAN_QA>(L, rs, offq, ct, tg); }
  if (MOD_LATE) load_tile128(W2, p.wmod, NMOD, 256 * ks, 2048 + 128 * cg, cw, lane);
  if (WO_LATE == 3) load_rows32(W, p.wo, 32 * a, cw, lane);
  SSTAMP(2);
  {
    const f32x4 s = reduce_to_wave0(L, cw, lane, gemv_tile128(L + L_Y, W2, dl, cw, lane), 3);
    if (cw == 0) put4(rs, WS_F3A + ((ks * 2 + (g >> 1)) * 1024 + 32 * cg + 2 * (lane & 15) + (g & 1)) * 16, s, tg);
  }
  if (cw == 0) {
    const int v = (lane >> 2) & 3, b = (lane >> 1) & 1, half = lane & 1;
    int off[1] = {WS_F3A + (((lane >> 4) * 2 + b) * 1024 + v * 256 + 2 * a + half) * 16};
    v4u gv[1];
    sweep<1, CAN_SLOW>(L, rs, off, gv, tg);
    f32x4 s = gval(gv[0]);
#pragma unroll
    for (int e = 0; e < 4; ++e) s[e] = sum_16_32(s[e]);
    const unsigned lo = pack2(s[0], s[1]), hi = pack2(s[2], s[3]);
    if (lane < 16) {
      if (v == 1 || v == 2) put(rs, WS_F3B + ((b * 2 + v - 1) * 256 + 2 * a + half) * 16, lo, hi, tg);
      else lds_st<f32x4>(L + L_GAB + ((v ? 1 : 0) * 4 + b * 2 + half) * 16, f32x4{bf_lo(lo), bf_hi(lo), bf_lo(hi), bf_hi(hi)});
    }
    SSTAMP(3);
    stats_exchange(L, rs, x, i, lane, tg);
  }
  SSTAMP(4);
  if (WO_LATE == 1) {
    flag_wait<4>(L, FL_LASTV, 4);
    load_rows32(W, p.wo, 32 * a, cw, lane);
  }
  int off1[8];
#pragma unroll
  for (int k = 0; k < 8; ++k) { off1[k] = WS_H1 + ((((((lane >> 3) & 1) * 32 + (a >> 2)) * 4 + (a & 3)) * 32 + 4 * k + g) * 8 + (lane & 7)) * 16; PIN(off1[k]); }
  int bp0 = (32 * ((lane >> 3) & 1) + g) * 4, bp1 = bp0 + 64;
  int off2 = WS_H2 + (lane >> 2) * 8192 + a * 64 + (lane & 3) * 16, off4 = off2 + (WS_H4 - WS_H2);
  PIN(bp0); PIN(bp1); PIN(off2); PIN(off4);
  v2u sel = sel_op(lane); PIN(sel);
  publish_partial(L, rs, x, i, ct, tg);
  SSTAMP(5);
  if (WO_LATE == 2) {
    flag_add(L, FL_LASTV);
    flag_wait<0>(L, FL_LASTV, 4);
    load_rows32(W, p.wo, 32 * a, cw, lane);
  }
  unsigned long long tpub;
  {
    v4u v[8];
    sweep<8, CAN_H1>(L, rs, off1, v, tg);
    SSTAMP(6);
    f32x4 s = {0.f, 0.f, 0.f, 0.f};
#pragma unroll
    for (int k = 0; k < 8; ++k) s = gsum_t(sel, v[k], s);
    const unsigned p01 = pack2(s[0], s[1]), p23 = pack2(s[2], s[3]);
    const v4u av = {(unsigned)__builtin_amdgcn_ds_bpermute(bp0, (int)p01), (unsigned)__builtin_amdgcn_ds_bpermute(bp0, (int)p23),
                    (unsigned)__builtin_amdgcn_ds_bpermute(bp1, (int)p01), (unsigned)__builtin_amdgcn_ds_bpermute(bp1, (int)p23)};
    f32x4 acc0, acc1;
    gemv_rows32(W, dl, av, acc0, acc1);
    put_owner(rs, WS_H2, a, cw, lane, acc0, acc1, tg);
    tpub = now10ns();
  }
  SSTAMP(7);
  if (cw < NPOLL) {
    const f32x4 ga = lds_ld<f32x4>(L + L_GAB + (lane & 3) * 16), gb = lds_ld<f32x4>(L + L_GAB + (4 + (lane & 3)) * 16);
    const f32x4 xv = {bf_lo(xown.x), bf_hi(xown.x), bf_lo(xown.y), bf_hi(xown.y)};
    int off3 = WS_H3 + (((lane >> 2) * 128 + a) * 8 + (lane & 3)) * 16;
    PIN(off3);
    delay_since(tpub, DLY_H2 + cw * STAG);
    const f32x4 attn = gather_owner<128, CAN_H2>(L, rs, off2, sel, tg);
    SSTAMP(8);
    f32x4 x1;
#pragma unroll
    for (int e = 0; e < 4; ++e) x1[e] = xv[e] + ga[e] * rbf(attn[e]);
    const unsigned x1lo = pack2(x1[0], x1[1]), x1hi = pack2(x1[2], x1[3]);
    if (NPOLL == 1 || flag_claim(L, FL_X1)) { if (lane < 4 * NREP3) put(rs, off3, x1lo, x1hi, tg); }
    tpub = now10ns();
    SSTAMP(9);
    const f32x4 x1r = {bf_lo(x1lo), bf_hi(x1lo), bf_lo(x1hi), bf_hi(x1hi)};
    const f32x4 bv = {bf_lo(b2own.x), bf_hi(b2own.x), bf_lo(b2own.y), bf_hi(b2own.y)};
    if (cw == 0) {
      delay_since(tpub, DLY_H4);
      const f32x4 mlp = gather_owner<112, CAN_H4>(L, rs, off4, sel, tg);
      SSTAMP(10);
      if (lane < 4) {
        f32x4 r;
#pragma unroll
        for (int e = 0; e < 4; ++e) r[e] = x1r[e] + gb[e] * (mlp[e] + bv[e]);
        if (*(volatile LAS unsigned*)(L + L_FLAG + FL_ABORT * 4)) r = f32x4{NAN, NAN, NAN, NAN};
        *(GAS v2u*)(p.out + (lane >> 1) * D + 8 * a + 4 * (lane & 1)) = v2u{pack2(r[0], r[1]), pack2(r[2], r[3])};
        if (a == 0 && lane == 0) __hip_atomic_store((GAS unsigned*)(p.ws + WS_EPOCH), tg.a, __ATOMIC_RELAXED, __HIP_MEMORY_SCOPE_AGENT);
      }
    }
  }
  SSTAMP(11);
  SSTAMP_FLUSH();
}

typedef float f32x2 __attribute__((ext_vector_type(2)));
__device__ __forceinline__ void ln_stats(f32x2 (&v)[8], float& mean, float& rstd) {
  const float shift = __builtin_bit_cast(float, __builtin_amdgcn_readfirstlane(__builtin_bit_cast(int, v[0][0])));
  const f32x2 sh = {shift, shift};
#pragma unroll
  for (int k = 0; k < 8; ++k) v[k] -= sh;
  const f32x2 s2 = ((v[0] + v[1]) + (v[2] + v[3])) + ((v[4] + v[5]) + (v[6] + v[7]));
  f32x2 qa = v[0] * v[0], qb = v[1] * v[1];
#pragma unroll
  for (int k = 2; k < 8; k += 2) { qa = __builtin_elementwise_fma(v[k], v[k], qa); qb = __builtin_elementwise_fma(v[k + 1], v[k + 1], qb); }
  qa += qb;
  float s = s2[0] + s2[1], q = qa[0] + qa[1];
  wave_sum2(s, q);
  mean = s * (1.0f / D);
  rstd = __builtin_amdgcn_rsqf(fmaxf(q * (1.0f / D) - mean * mean, 0.f) + LN_EPS);
}
__device__ __forceinline__ void chain_b(const Params& p, lptr L, __amdgpu_buffer_rsrc_t rs, int x, int i, int cw, int lane TPARAM) {
  SSTAMP_DECL;
  const int j = 16 * x + (i - 16), ct = cw * 64 + lane, g = lane >> 4;
  const int ks = x & 3, cgrp = (x >> 2) * 16 + (i - 16);
  const int b = cw >> 1, gi = 32 * (cw & 1) + (lane & 31);
  v4u W[16], W2[16];
  v4u xa = *(const GAS v4u*)(p.x + b * D + 16 * lane), xb = *(const GAS v4u*)(p.x + b * D + 16 * lane + 8);
  unsigned xs = *(const GAS unsigned*)(p.x + b * D + 256 * ks + 4 * gi + 2 * (lane >> 5));
  Tag tg = {__hip_atomic_load((GAS unsigned*)(p.ws + WS_EPOCH), __ATOMIC_RELAXED, __HIP_MEMORY_SCOPE_AGENT), 0u};
  __builtin_amdgcn_sched_barrier(0);
  if (WQ_DELAY) __builtin_amdgcn_s_sleep(WQ_DELAY);
  load_tile128(W, p.wq, 4096, 256 * ks, 128 * cgrp, cw, lane);
  SSTAMP(12);
  unsigned b1u = *(const GAS u16*)(p.b1 + 32 * (j < 112 ? j : 0) + (lane & 31));
  __builtin_amdgcn_sched_barrier(0);
  const Delta dl = make_delta(lane);
  PIN(xa); PIN(xb); PIN(xs); PIN(tg.a);
  SSTAMP(13);
  tg.a += 1u; tg.b = ~tg.a;
  {
    f32x4 xv[4];
#pragma unroll
    for (int e = 0; e < 2; ++e) {
      xv[e] = f32x4{bf_lo(xa[2 * e]), bf_hi(xa[2 * e]), bf_lo(xa[2 * e + 1]), bf_hi(xa[2 * e + 1])};
      xv[2 + e] = f32x4{bf_lo(xb[2 * e]), bf_hi(xb[2 * e]), bf_lo(xb[2 * e + 1]), bf_hi(xb[2 * e + 1])};
    }
    const float shift = __builtin_bit_cast(float, __builtin_amdgcn_readfirstlane(__builtin_bit_cast(int, xv[0][0])));
    float s = 0.f, q = 0.f;
#pragma unroll
    for (int k = 0; k < 4; ++k)
#pragma unroll
      for (int e = 0; e < 4; ++e) { const float d = xv[k][e] - shift; s += d; q += d * d; }
    wave_sum2(s, q);
    const float mean = s * (1.0f / D);
    const float rstd = __builtin_amdgcn_rsqf(fmaxf(q * (1.0f / D) - mean * mean, 0.f) + LN_EPS);
    float xn0 = (bf_lo(xs) - shift - mean) * rstd, xn1 = (bf_hi(xs) - shift - mean) * rstd;
    PIN(xn0); PIN(xn1);
    int off[4]; v4u v[4];
#pragma unroll
    for (int k = 0; k < 4; ++k) { off[k] = WS_F1 + ((k * 2 + b) * 512 + (lane >> 5) * 256 + 64 * ks + gi) * 16; PIN(off[k]); }
    int ao = L_ACT + (((b * 4 + (gi >> 4)) * 4 + 2 * (lane >> 5)) * 16 + (gi & 15)) * 2;
    PIN(ao);
    sweep<4, CAN_FRONT>(L, rs, off, v, tg);
    SSTAMP(15);
    if (W2FRONT == 1 && j < 112) load_rows32(W2, p.w2, 32 * j, cw, lane);
    const f32x4 t = (gval(v[0]) + gval(v[1])) + (gval(v[2]) + gval(v[3]));
    const unsigned lo = pack2(t[0], t[1]), hi = pack2(t[2], t[3]);
    const auto slo = __builtin_amdgcn_permlane32_swap(lo, lo, false, false), shi = __builtin_amdgcn_permlane32_swap(hi, hi, false, false);
    const unsigned sa = lane < 32 ? slo[0] : shi[0], ca = lane < 32 ? slo[1] : shi[1];
    const unsigned hp = pack2(xn0 * (1.0f + bf_lo(ca)) + bf_lo(sa), xn1 * (1.0f + bf_hi(ca)) + bf_hi(sa));
    lds_st<u16>(L + ao, (u16)hp);
    lds_st<u16>(L + ao + 32, (u16)(hp >> 16));
  }
  int offq[4];
  q_offsets(offq, x, ct);
  int f2off = WS_F2 + ((ks * 2 + (g >> 1)) * 1024 + 32 * cgrp + 2 * (lane & 15) + (g & 1)) * 16;
  PIN(f2off);
  wg_barrier();
  SSTAMP(1);
  {
    f32x4 s = gemv_tile128(L + L_ACT, W, dl, cw, lane);
    if (cw != 0) lds_st<f32x4>(L + L_RED + cw * 1024 + lane * 16, s);
    wg_barrier();
    if (cw == 0) {
#pragma unroll
      for (int ww = 1; ww < 4; ++ww) s += lds_ld<f32x4>(L + L_RED + ww * 1024 + lane * 16);
      put4(rs, f2off, s, tg);
    }
    if (W2FRONT == 2 && j < 112) load_rows32(W2, p.w2, 32 * j, cw, lane);
  }
  const unsigned long long tq = now10ns();
  SSTAMP(2);
  delay_since(tq, DLY_QB);
  gather_q<CAN_QB>(L, rs, offq, ct, tg);
  PIN(b1u);
  SSTAMP(3);
  if (cw == 0) stats_exchange(L, rs, x, i, lane, tg);
  if (W2EARLY && j < 112) {
    const int c = lane & 15;
    flag_wait<8>(L, FL_STAT, W2EARLY);
#pragma unroll
    for (int u = 0; u < 4; ++u) {
      if (u >= 1) WAIT_VM(0);
#pragma unroll
      for (int v = 0; v < 4; ++v) { const int e = 4 * u + v; W2[e] = ldw(p.w2 + (size_t)(32 * j + 4 * (e & 7) + g) * D + (2 * cw + (e >> 3)) * 128 + 8 * c); }
      __builtin_amdgcn_sched_barrier(0);
    }
  }
  SSTAMP(5);
  int off3[4];
  if (j < 112) {
    int off[4]; v4u v[4];
#pragma unroll
    for (int k = 0; k < 4; ++k) off[k] = WS_F3B + (k * 256 + ct) * 16;
    sweep<4, CAN_SLOW>(L, rs, off, v, tg);
#pragma unroll
    for (int k = 0; k < 4; ++k) lds_st<v2u>(L + L_SBCB + (k * 1024 + 4 * ct) * 2, v2u{v[k].x, v[k].z});
#pragma unroll
    for (int k = 0; k < 4; ++k) { off3[k] = WS_H3 + ((x * 128 + (k >> 1) * 64 + lane) * 8 + 2 * (cw & 1) + (k & 1)) * 16; PIN(off3[k]); }
  }
  unsigned voW[4];
#pragma unroll
  for (int v = 0; v < 4; ++v) { voW[v] = (16 * v + (lane >> 2)) * (FF * 2) + (((lane & 3) ^ (((lane >> 4) & 1) << 1)) << 4) - v * 1024; PIN(voW[v]); }
  SSTAMP(6);
  publish_partial(L, rs, x, i, ct, tg);
  SSTAMP(7);
  if (j >= 112) { SSTAMP_FLUSH(); return; }
  if (W12_DELAY) __builtin_amdgcn_s_sleep(W12_DELAY);
  {
    const char* W1b = (const char*)p.w1 + (size_t)(256 * cw) * (FF * 2) + j * 64;
    const unsigned ring = (unsigned)(size_t)L + L_RING + cw * 32768;
#pragma unroll
    for (int u = 0; u < 4; ++u) {
      const char* b_ = W1b + (size_t)u * 64 * (FF * 2); const unsigned la_ = ring + u * 4096;
      if (u >= PACE) WAIT_VM(4 * (PACE - 1));
      if (!NOW12) DMA_TILE(voW[0], voW[1], voW[2], voW[3], b_, la_);
    }
    const int c = lane & 15;
#pragma unroll
    for (int u = 0; u < (W2EARLY || NOW12 || W2FRONT ? 0 : 4); ++u) {
      if (4 + u >= PACE) WAIT_VM(4 * (PACE - 1));
#pragma unroll
      for (int v = 0; v < 4; ++v) { const int e = 4 * u + v; W2[e] = ldw(p.w2 + (size_t)(32 * j + 4 * (e & 7) + g) * D + (2 * cw + (e >> 3)) * 128 + 8 * c); }
      __builtin_amdgcn_sched_barrier(0);
    }
  }
  WAIT_LGKM0();
  __builtin_amdgcn_s_barrier();
  WAIT_VM(W2EARLY || NOW12 || W2FRONT ? 0 : 16);
  v4u w1f[8][2];
  {
    const int q = (lane & 15) >> 2, pp = lane & 3;
#pragma unroll
    for (int kk = 0; kk < 8; ++kk)
#pragma unroll
      for (int ch = 0; ch < 2; ++ch) {
        v2u w[2];
#pragma unroll
        for (int hh = 0; hh < 2; ++hh) {
          const int row = (kk & 1) * 32 + 16 * hh + 4 * g + q;
          const int pc = (2 * ch + (pp >> 1)) ^ (((row >> 2) & 1) << 1);
          w[hh] = lds_tr(L + L_RING + cw * 32768 + (kk >> 1) * 4096 + row * 64 + pc * 16 + (pp & 1) * 8);
        }
        w1f[kk][ch] = v4u{w[0].x, w[0].y, w[1].x, w[1].y};
        PIN(w1f[kk][ch]);
      }
  }
  if (cw < 2 * LNPOLL) {
    const int cwb = cw & 1;
    f32x2 sb[8], cb[8];
    int ypos[2];
#pragma unroll
    for (int k = 0; k < 4; ++k) {
      const int col = 8 * ((k >> 1) * 64 + lane) + 4 * (k & 1);
      const v2u s = lds_ld<v2u>(L + L_SBCB + ((cwb * 2 + 0) * 1024 + col) * 2), c = lds_ld<v2u>(L + L_SBCB + ((cwb * 2 + 1) * 1024 + col) * 2);
      sb[2 * k] = f32x2{bf_lo(s.x), bf_hi(s.x)}; sb[2 * k + 1] = f32x2{bf_lo(s.y), bf_hi(s.y)};
      cb[2 * k] = f32x2{1.0f + bf_lo(c.x), 1.0f + bf_hi(c.x)}; cb[2 * k + 1] = f32x2{1.0f + bf_lo(c.y), 1.0f + bf_hi(c.y)};
      PIN(sb[2 * k]); PIN(sb[2 * k + 1]); PIN(cb[2 * k]); PIN(cb[2 * k + 1]);
    }
#pragma unroll
    for (int k = 0; k < 2; ++k) {
      const int gq = 2 * (k * 64 + lane);
      ypos[k] = L_Y + (cwb * D + (gq >> 3) * 32 + (gq & 3) * 8 + ((gq >> 2) & 1) * 4) * 2;
      PIN(ypos[k]);
    }
    v4u v[4];
    if (cw >= 2) __builtin_amdgcn_s_sleep(LNSTAG);
    sweep<4, CAN_H3>(L, rs, off3, v, tg);
    SSTAMP(8);
    if (LNPOLL == 1 || flag_claim(L, FL_LN0 + cwb)) {
      f32x2 y[8];
#pragma unroll
      for (int k = 0; k < 4; ++k) { y[2 * k] = f32x2{bf_lo(v[k].x), bf_hi(v[k].x)}; y[2 * k + 1] = f32x2{bf_lo(v[k].z), bf_hi(v[k].z)}; }
      float mean, rstd;
      ln_stats(y, mean, rstd);
      {
        const f32x2 r2 = {rstd, rstd}, m2 = {mean, mean};
#pragma unroll
        for (int k = 0; k < 8; ++k) y[k] = __builtin_elementwise_fma((y[k] - m2) * r2, cb[k], sb[k]);
      }
      ln_stats(y, mean, rstd);
      {
        const f32x2 r2 = {rstd, rstd}, c2 = {-mean * rstd, -mean * rstd};
#pragma unroll
        for (int k = 0; k < 8; ++k) y[k] = __builtin_elementwise_fma(y[k], r2, c2);
      }
#pragma unroll
      for (int k = 0; k < 2; ++k) {
        lds_st<v2u>(L + ypos[k], v2u{pack2(y[4 * k][0], y[4 * k][1]), pack2(y[4 * k + 1][0], y[4 * k + 1][1])});
        lds_st<v2u>(L + ypos[k] + 16, v2u{pack2(y[4 * k + 2][0], y[4 * k + 2][1]), pack2(y[4 * k + 3][0], y[4 * k + 3][1])});
      }
    }
  }
  WAIT_LGKM0();
  __builtin_amdgcn_s_barrier();
  SSTAMP(9);
  {
    f32x4 acc[2] = {{0.f, 0.f, 0.f, 0.f}, {0.f, 0.f, 0.f, 0.f}};
#pragma unroll
    for (int kk = 0; kk < 8; ++kk) {
      const v4u ya = lds_ld<v4u>(L + L_Y + ((lane & 1) * D + 256 * cw + 32 * kk + 8 * g) * 2);
      acc[0] = mfma32(ya, w1f[kk][0], acc[0]);
      acc[1] = mfma32(ya, w1f[kk][1], acc[1]);
    }
    if (lane < 16) {
#pragma unroll
      for (int ch = 0; ch < 2; ++ch)
#pragma unroll
        for (int bb = 0; bb < 2; ++bb) lds_st<float>(L + L_RED + (cw * 64 + bb * 32 + ch * 16 + lane) * 4, acc[ch][bb]);
    }
    WAIT_LGKM0();
    __builtin_amdgcn_s_barrier();
    float a = bf_lo(b1u);
#pragma unroll
    for (int ww = 0; ww < 4; ++ww) a += lds_ld<float>(L + L_RED + (ww * 64 + lane) * 4);
    const float hv = gelu_tanh(rbf(a));
    const int k = lane & 31;
    lds_st<u16>(L + L_HID + cw * 128 + ((lane >> 5) * 32 + (k & 3) * 8 + (k >> 2)) * 2, (u16)pack2(hv, hv));
    const v4u av = lds_ld<v4u>(L + L_HID + cw * 128 + (((lane >> 3) & 1) * 32 + g * 8) * 2);
    f32x4 acc0, acc1;
    gemv_rows32(W2, dl, av, acc0, acc1);
    put_owner(rs, WS_H4, j, cw, lane, acc0, acc1, tg);
  }
  SSTAMP(10);
  SSTAMP_FLUSH();
}

__global__ void __launch_bounds__(512, 2) fused_decode_block(Params p) {
  extern __shared__ __attribute__((aligned(16))) unsigned char smem[];
  lptr L = (lptr)smem;
  const int tid = threadIdx.x, lane = tid & 63, wave = __builtin_amdgcn_readfirstlane(tid >> 6);
  const int x = blockIdx.x & 7, ib = blockIdx.x >> 3;
  const int i = ROLEMAP ? (((ib >> 2) & 1) << 4) | ((ib >> 3) << 2) | (ib & 3) : ib;
#define TENTRY
  if (wave < 4) {
    if (tid < 16) { *(LAS unsigned*)(L + L_FLAG + tid * 4) = 0u; WAIT_LGKM0(); }
    if (i < 16) stream_wave<NT_A, 8, PW_A, LA_A, 1, 0>(p, L, x, i, wave, lane TENTRY);
    else stream_wave<32 - NT_A, (PRE_B + LA_B_EXTRA > LA_B ? PRE_B + LA_B_EXTRA : LA_B), LA_B, LA_B, 3, PRE_B>(p, L, x, i, wave, lane TENTRY);
    return;
  }
  __builtin_amdgcn_s_setprio(2);
  const __amdgpu_buffer_rsrc_t rs = __builtin_amdgcn_make_buffer_rsrc((void*)p.ws, 0, WS_END, 0x00020000);
  if (i < 16) chain_a(p, L, rs, x, i, wave - 4, lane TENTRY);
  else chain_b(p, L, rs, x, i, wave - 4, lane TENTRY);
}

}

extern "C" void kernel_launch(void* const* d_in, const int* in_sizes, int n_in, void* d_out, int out_size, void* d_ws, size_t ws_size,
                              hipStream_t stream) {
  Params p;
  p.x = (const u16*)d_in[0];
  p.K = (const u16*)d_in[1];
  p.V = (const u16*)d_in[2];
  p.wq = (const u16*)d_in[3];
  p.wo = (const u16*)d_in[4];
  p.w1 = (const u16*)d_in[6];
  p.b1 = (const u16*)d_in[7];
  p.w2 = (const u16*)d_in[8];
  p.b2 = (const u16*)d_in[9];
  p.c = (const u16*)d_in[10];
  p.wmod = (const u16*)d_in[11];
  p.out = (u16*)d_out;
  p.ws = (unsigned char*)d_ws;
  (void)hipFuncSetAttribute((const void*)fused_decode_block, hipFuncAttributeMaxDynamicSharedMemorySize, LDS_BYTES);
  hipLaunchKernelGGL(fused_decode_block, dim3(256), dim3(512), LDS_BYTES, stream, p);
}
```

```cpp
#include <hip/hip_runtime.h>
#include <hip/hip_bf16.h>
#include <stdint.h>

#define GAS __attribute__((address_space(1)))
#define LAS __attribute__((address_space(3)))
typedef unsigned v4u __attribute__((ext_vector_type(4)));
typedef unsigned v2u __attribute__((ext_vector_type(2)));
typedef float f32x4 __attribute__((ext_vector_type(4)));
typedef float f32x2 __attribute__((ext_vector_type(2)));
typedef __bf16 bf16x8 __attribute__((ext_vector_type(8)));
typedef __bf16 bf16x2 __attribute__((ext_vector_type(2)));
typedef short s16x4 __attribute__((ext_vector_type(4)));
typedef unsigned short u16;
typedef LAS unsigned char* lptr;

namespace {

constexpr int D = 1024, FF = 3584, NMOD = 6144;
constexpr float SC_LOG2 = 0.08838834764831845f * 1.4426950408889634f;
constexpr float LN_EPS = 1e-5f;

constexpr int NT_A = 17, NT_B = 32 - NT_A;
constexpr int PW_A = 4;
constexpr int LA_A = 3, LA_B = 4;
constexpr int LA_SM = 6;
constexpr int PACE = 2;

constexpr int WS_EPOCH = 0;
constexpr int WS_F1  = 4096;
constexpr int WS_F2  = WS_F1 + 4096 * 16;
constexpr int WS_F3A = WS_F2 + 8192 * 16;
constexpr int WS_F3B = WS_F3A + 8192 * 16;
constexpr int WS_ST  = WS_F3B + 1024 * 16;
constexpr int WS_H1  = WS_ST + 2048 * 16;
constexpr int WS_H2  = WS_H1 + 65536 * 16;
constexpr int NREP3  = 8;
constexpr int WS_H3  = WS_H2 + 65536 * 16;
constexpr int WS_H4  = WS_H3 + NREP3 * 1024 * 16;
constexpr int WS_END = WS_H4 + 65536 * 16;

constexpr int L_RING = 0;
constexpr int L_ACT  = 131072;
constexpr int L_Q    = L_ACT + 1024;
constexpr int L_RED  = L_Q + 2048;
constexpr int L_SBCB = L_RED + 4096;
constexpr int L_Y    = L_SBCB + 8192;
constexpr int L_STAT = L_Y + 4096;
constexpr int L_FAC  = L_STAT + 256;
constexpr int L_GAB  = L_FAC + 128;
constexpr int L_HID  = L_GAB + 128;
constexpr int L_FLAG = L_HID + 512;
constexpr int LDS_BYTES = 163840;
static_assert(L_FLAG + 64 <= LDS_BYTES, "LDS map");
enum { FL_STAT, FL_FAC, FL_ODONE, FL_RED, FL_ABORT };

constexpr unsigned SPIN_GLOBAL = 1u << 16;
constexpr unsigned SPIN_LDS = 1u << 21;

struct Params {
  const u16 *x, *K, *V, *wq, *wo, *w1, *b1, *w2, *b2, *c, *wmod;
  u16* out;
  unsigned char* ws;
};

#define PIN(x) asm volatile("" : "+v"(x))
#define WAIT_VM(n) asm volatile("s_waitcnt vmcnt(%0)" ::"i"(n) : "memory")
#define WAIT_LGKM0() asm volatile("s_waitcnt lgkmcnt(0)" ::: "memory")
__device__ __forceinline__ float bf_lo(unsigned u) { return __builtin_bit_cast(float, u << 16); }
__device__ __forceinline__ float bf_hi(unsigned u) { return __builtin_bit_cast(float, u & 0xffff0000u); }
__device__ __forceinline__ unsigned pack2(float lo, float hi) { bf16x2 r = {(__bf16)lo, (__bf16)hi}; return __builtin_bit_cast(unsigned, r); }
__device__ __forceinline__ float rbf(float v) { return bf_lo(pack2(v, v)); }
template <int CTRL> __device__ __forceinline__ float dpp(float v) {
  return __builtin_bit_cast(float, __builtin_amdgcn_mov_dpp(__builtin_bit_cast(int, v), CTRL, 0xf, 0xf, true));
}
constexpr int XOR1 = 0xB1, XOR2 = 0x4E, ROR4 = 0x124, ROR8 = 0x128;
__device__ __forceinline__ float sum_16_32(float v) {
  auto s = __builtin_amdgcn_permlane16_swap(__float_as_uint(v), __float_as_uint(v), false, false);
  v = __uint_as_float(s[0]) + __uint_as_float(s[1]);
  auto t = __builtin_amdgcn_permlane32_swap(__float_as_uint(v), __float_as_uint(v), false, false);
  return __uint_as_float(t[0]) + __uint_as_float(t[1]);
}
__device__ __forceinline__ float max_16_32(float v) {
  auto s = __builtin_amdgcn_permlane16_swap(__float_as_uint(v), __float_as_uint(v), false, false);
  v = fmaxf(__uint_as_float(s[0]), __uint_as_float(s[1]));
  auto t = __builtin_amdgcn_permlane32_swap(__float_as_uint(v), __float_as_uint(v), false, false);
  return fmaxf(__uint_as_float(t[0]), __uint_as_float(t[1]));
}
__device__ __forceinline__ void wave_sum2(float& a, float& b) {
  a += dpp<XOR1>(a); b += dpp<XOR1>(b);
  a += dpp<XOR2>(a); b += dpp<XOR2>(b);
  a += dpp<ROR4>(a); b += dpp<ROR4>(b);
  a += dpp<ROR8>(a); b += dpp<ROR8>(b);
  a = sum_16_32(a); b = sum_16_32(b);
}
__device__ __forceinline__ f32x4 mfma32(v4u a, v4u b, f32x4 c) {
  return __builtin_amdgcn_mfma_f32_16x16x32_bf16(__builtin_bit_cast(bf16x8, a), __builtin_bit_cast(bf16x8, b), c, 0, 0, 0);
}
__device__ __forceinline__ f32x4 mfma16(v2u a, v2u b, f32x4 c) {
  return __builtin_amdgcn_mfma_f32_16x16x16bf16_1k(__builtin_bit_cast(s16x4, a), __builtin_bit_cast(s16x4, b), c, 0, 0, 0);
}
__device__ __forceinline__ v2u lds_tr(lptr p) {
  return __builtin_bit_cast(v2u, __builtin_amdgcn_ds_read_tr16_b64_v4i16((LAS s16x4*)p));
}
template <class T> __device__ __forceinline__ T lds_ld(lptr p) { return *(LAS T*)p; }
template <class T> __device__ __forceinline__ void lds_st(lptr p, T v) { *(LAS T*)p = v; }
__device__ __forceinline__ v4u ldw(const u16* p) { return __builtin_nontemporal_load((const GAS v4u*)p); }
__device__ __forceinline__ void delay_since(unsigned long long t0, int ticks) {
  while (__builtin_amdgcn_s_memrealtime() - t0 < (unsigned long long)ticks) {}
}
__device__ __forceinline__ float silu(float v) { return v * __builtin_amdgcn_rcpf(1.0f + __expf(-v)); }
__device__ __forceinline__ float gelu_tanh(float a) {
  const float u = 0.7978845608028654f * (a + 0.044715f * a * a * a);
  const float e = __expf(2.0f * u);
  const float th = 1.0f - 2.0f * __builtin_amdgcn_rcpf(e + 1.0f);
  return 0.5f * a * (1.0f + th);
}

__device__ __forceinline__ void flag_add(lptr L, int f) {
  unsigned addr = (unsigned)(size_t)L + L_FLAG + f * 4;
  unsigned one = 1u;
  unsigned long long keep;
  asm volatile("s_waitcnt lgkmcnt(0)\n\ts_mov_b64 %0, exec\n\ts_mov_b64 exec, 1\n\tds_add_u32 %1, %2\n\ts_mov_b64 exec, %0"
               : "=&s"(keep) : "v"(addr), "v"(one) : "memory");
}
template <int SLEEP>
__device__ __forceinline__ void flag_wait(lptr L, int f, unsigned want) {
  volatile LAS unsigned* fl = (volatile LAS unsigned*)(L + L_FLAG);
#pragma nounroll
  for (unsigned sp = 0; fl[f] < want; ++sp) {
    if (sp > SPIN_LDS) { fl[FL_ABORT] = 1; break; }
    if (SLEEP) __builtin_amdgcn_s_sleep(SLEEP);
  }
  asm volatile("" ::: "memory");
}
__device__ __forceinline__ void wg_barrier() { WAIT_LGKM0(); __builtin_amdgcn_s_barrier(); asm volatile("" ::: "memory"); }

struct Tag { unsigned a, b; };
__device__ __forceinline__ void put(__amdgpu_buffer_rsrc_t rs, int off, unsigned lo, unsigned hi, Tag t) {
  const v4u g = {lo, t.a, hi, t.b};
  __builtin_amdgcn_raw_buffer_store_b128(g, rs, off, 0, 16);
}
__device__ __forceinline__ void put4(__amdgpu_buffer_rsrc_t rs, int off, f32x4 v, Tag t) { put(rs, off, pack2(v[0], v[1]), pack2(v[2], v[3]), t); }
template <int NL, int CAN>
__device__ __forceinline__ void sweep(lptr L, __amdgpu_buffer_rsrc_t rs, const int (&off)[NL], v4u (&v)[NL], Tag t) {
  if (CAN > 0) {
    const int oc = __builtin_amdgcn_readlane(off[NL - 1], 63);
#pragma nounroll
    for (unsigned sp = 0;; ++sp) {
      const v4u c = __builtin_amdgcn_raw_buffer_load_b128(rs, 0, oc, 16);
      if (__all((c.y == t.a) & (c.w == t.b))) break;
      if (sp > SPIN_GLOBAL) break;
      if (CAN > 1) __builtin_amdgcn_s_sleep(CAN - 1);
      asm volatile("" ::: "memory");
    }
  }
#pragma nounroll
  for (unsigned sp = 0;; ++sp) {
#pragma unroll
    for (int k = 0; k < NL; ++k) v[k] = __builtin_amdgcn_raw_buffer_load_b128(rs, off[k], 0, 16);
    bool ok = true;
#pragma unroll
    for (int k = 0; k < NL; ++k) ok &= (v[k].y == t.a) & (v[k].w == t.b);
    if (__all(ok)) break;
    if (sp > SPIN_GLOBAL) { *(volatile LAS unsigned*)(L + L_FLAG + FL_ABORT * 4) = 1; break; }
    asm volatile("" ::: "memory");
  }
}
__device__ __forceinline__ f32x4 gval(v4u g) { return f32x4{bf_lo(g.x), bf_hi(g.x), bf_lo(g.z), bf_hi(g.z)}; }
__device__ __forceinline__ v2u sel_op(int lane) {
  const int m = lane & 3;
  return v2u{m == 0 ? 0x00003f80u : m == 1 ? 0x3f800000u : 0u, m == 2 ? 0x00003f80u : m == 3 ? 0x3f800000u : 0u};
}
__device__ __forceinline__ f32x4 gsum(v2u sel, v4u g, f32x4 acc) { return mfma16(sel, v2u{g.x, g.z}, acc); }
__device__ __forceinline__ f32x4 gsum_t(v2u sel, v4u g, f32x4 acc) { return mfma16(v2u{g.x, g.z}, sel, acc); }

struct Delta { unsigned m[4]; };
__device__ __forceinline__ Delta make_delta(int lane) {
  const int j = lane & 7;
  Delta d;
#pragma unroll
  for (int r = 0; r < 4; ++r) { d.m[r] = (r == (j >> 1)) ? ((j & 1) ? 0xffff0000u : 0x0000ffffu) : 0u; PIN(d.m[r]); }
  return d;
}
__device__ __forceinline__ v4u delta_op(const Delta& d, unsigned pair, int hi) {
  const unsigned dup = __builtin_amdgcn_perm(pair, pair, hi ? 0x03020302u : 0x01000100u);
  return v4u{dup & d.m[0], dup & d.m[1], dup & d.m[2], dup & d.m[3]};
}
__device__ __forceinline__ void load_tile128(v4u (&W)[16], const u16* Wg, int ld, int row0, int col0, int cw, int lane) {
  const int g = lane >> 4, c = lane & 15;
#pragma unroll
  for (int u = 0; u < 16; ++u) W[u] = ldw(Wg + (size_t)(row0 + 64 * cw + 4 * u + g) * ld + col0 + 8 * c);
  __builtin_amdgcn_sched_barrier(0);
}
__device__ __forceinline__ f32x4 gemv_tile128(lptr act, const v4u (&W)[16], const Delta& dl, int cw, int lane) {
  const int g = lane >> 4, b = (lane >> 3) & 1;
  const v4u a0 = lds_ld<v4u>(act + ((b * 4 + cw) * 4 + g) * 32), a1 = lds_ld<v4u>(act + ((b * 4 + cw) * 4 + g) * 32 + 16);
  f32x4 acc0 = {0.f, 0.f, 0.f, 0.f}, acc1 = acc0;
#pragma unroll
  for (int u = 0; u < 8; ++u) {
    acc0 = mfma32(delta_op(dl, a0[u >> 1], u & 1), W[u], acc0);
    acc1 = mfma32(delta_op(dl, a1[u >> 1], u & 1), W[8 + u], acc1);
  }
  return acc0 + acc1;
}
__device__ __forceinline__ const u16* rows32_ptr(const u16* Wg, int row0, int cw, int lane, int e) {
  return Wg + (size_t)(row0 + 4 * (e & 7) + (lane >> 4)) * D + (2 * cw + (e >> 3)) * 128 + 8 * (lane & 15);
}
__device__ __forceinline__ void gemv_rows32(const v4u (&W)[16], const Delta& dl, v4u av, f32x4& acc0, f32x4& acc1) {
  acc0 = f32x4{0.f, 0.f, 0.f, 0.f}; acc1 = acc0;
#pragma unroll
  for (int u = 0; u < 8; ++u) {
    const v4u a = delta_op(dl, av[u >> 1], u & 1);
    acc0 = mfma32(a, W[u], acc0);
    acc1 = mfma32(a, W[8 + u], acc1);
  }
}
__device__ __forceinline__ int owner_put_off(int base, int prod, int cw, int lane) { return base + prod * 8192 + (32 * cw + (lane & 15)) * 64 + (lane >> 4) * 16; }
template <int NP, int CAN>
__device__ __forceinline__ f32x4 gather_owner(lptr L, __amdgpu_buffer_rsrc_t rs, int off0, v2u sel, Tag tg) {
  constexpr int NL = NP / 16;
  int off[NL]; v4u v[NL];
#pragma unroll
  for (int k = 0; k < NL; ++k) off[k] = off0 + k * 16 * 8192;
  sweep<NL, CAN>(L, rs, off, v, tg);
  f32x4 s = {0.f, 0.f, 0.f, 0.f};
#pragma unroll
  for (int k = 0; k < NL; ++k) s = gsum(sel, v[k], s);
#pragma unroll
  for (int e = 0; e < 4; ++e) { float a = s[e]; a += dpp<ROR4>(a); s[e] = a + dpp<ROR8>(a); }
  return s;
}

#define DMA_TILE(o0, o1, o2, o3, base, ldsaddr, POL)                                                     \
  do {                                                                                                   \
    unsigned keep_;                                                                                      \
    asm volatile("s_nop 4\n\ts_mov_b32 %0, m0\n\ts_mov_b32 m0, %6\n\ts_nop 0\n\t"                        \
                 "global_load_lds_dwordx4 %1, %5" POL "\n\t"                                             \
                 "global_load_lds_dwordx4 %2, %5 offset:1024" POL "\n\t"                                 \
                 "global_load_lds_dwordx4 %3, %5 offset:2048" POL "\n\t"                                 \
                 "global_load_lds_dwordx4 %4, %5 offset:3072" POL "\n\t"                                 \
                 "s_mov_b32 m0, %0"                                                                      \
                 : "=&s"(keep_)                                                                          \
                 : "v"(o0), "v"(o1), "v"(o2), "v"(o3), "s"(base), "s"(ldsaddr)                           \
                 : "memory");                                                                            \
  } while (0)

constexpr int last_issued(int t, int npre, int la, int nt) {
  int li = npre - 1;
  for (int s = 0; s <= t; ++s) {
    int want = s + ((s >= nt - 3 && s <= nt - 1 && LA_SM > la) ? LA_SM : la);
    if (npre > la) {
      const int w2 = s + 8 < npre - 1 + la ? s + 8 : npre - 1 + la;
      if (w2 > want) want = w2;
    }
    if (want > s + 8) want = s + 8;
    if (want > 2 * nt - 1) want = 2 * nt - 1;
    if (want > li) li = want;
  }
  return li;
}
template <bool SET_A>
__device__ __forceinline__ void stream_wave(const Params& p, lptr L, int x, int i, int w, int lane) {
  constexpr int NT = SET_A ? NT_A : NT_B, LA = SET_A ? LA_A : LA_B, NPRE = SET_A ? 8 : LA_B, PW = SET_A ? PW_A : LA_B;
  const int g = lane >> 4, n = lane & 15;
  const size_t tile0 = (size_t)x * 2048 + 4 * i + w;
  const char* Kb = (const char*)p.K + tile0 * 4096;
  const char* Vb = (const char*)p.V + tile0 * 4096;
#define TOFF(t) ((t) < NT_B ? (size_t)(t) * 128 * 4096 : ((size_t)NT_B * 128 + (size_t)((t)-NT_B) * 64) * 4096)
  const unsigned ring = (unsigned)(size_t)L + L_RING + w * 32768;
  unsigned voK[4], voV[2];
#pragma unroll
  for (int v = 0; v < 4; ++v) voK[v] = g * 256 + ((n ^ (4 * v + g)) << 4);
#pragma unroll
  for (int v = 0; v < 2; ++v) voV[v] = g * 256 + ((n ^ (((4 * v + g) & 7) << 1)) << 4);
#define ISSUE(t)                                                                                                    \
  do {                                                                                                              \
    const unsigned la_ = ring + ((t)&7) * 4096;                                                                     \
    if ((t) < NT) { const char* b_ = Kb + TOFF(t); DMA_TILE(voK[0], voK[1], voK[2], voK[3], b_, la_, " nt"); }      \
    else { const char* b_ = Vb + TOFF((t)-NT); DMA_TILE(voV[0], voV[1], voV[0], voV[1], b_, la_, " nt"); }          \
  } while (0)
#define LI(t) last_issued(t, NPRE, LA, NT)
#define REFILL(t) do { _Pragma("unroll") for (int u_ = LI((t)-1) + 1; u_ <= LI(t); ++u_) ISSUE(u_); } while (0)

  __builtin_amdgcn_s_barrier();
  if (!SET_A) { __builtin_amdgcn_s_barrier(); __builtin_amdgcn_s_barrier(); }
#pragma unroll
  for (int t = 0; t < PW; ++t) ISSUE(t);
#pragma unroll
  for (int t = PW; t < NPRE; ++t) { WAIT_VM(4 * (PW - 1)); ISSUE(t); }
  if (SET_A) __builtin_amdgcn_s_barrier();
  asm volatile("" ::: "memory");

  v4u qf[4];
#pragma unroll
  for (int t4 = 0; t4 < 4; ++t4) qf[t4] = lds_ld<v4u>(L + L_Q + (n & 7) * 256 + t4 * 64 + g * 16);
  lptr rb = L + L_RING + w * 32768;
  int ka[4];
#pragma unroll
  for (int t4 = 0; t4 < 4; ++t4) ka[t4] = n * 256 + (((4 * t4 + g) ^ n) << 4);

  f32x4 sc[NT];
#pragma unroll
  for (int t = 0; t < NT; ++t) {
    WAIT_VM(4 * (LI(t - 1) - t));
    v4u kf[4];
#pragma unroll
    for (int t4 = 0; t4 < 4; ++t4) kf[t4] = lds_ld<v4u>(rb + (t & 7) * 4096 + ka[t4]);
    WAIT_LGKM0();
    REFILL(t);
    f32x4 a = {0.f, 0.f, 0.f, 0.f};
#pragma unroll
    for (int t4 = 0; t4 < 4; ++t4) a = mfma32(kf[t4], qf[t4], a);
    sc[t] = a;
  }
  float m = -INFINITY;
#pragma unroll
  for (int t = 0; t < NT; ++t) m = fmaxf(fmaxf(fmaxf(sc[t][0], sc[t][1]), fmaxf(sc[t][2], sc[t][3])), m);
  m = max_16_32(m);
  const float mm = m * SC_LOG2;
  float lsum = 0.f;
  v2u pb[NT];
#pragma unroll
  for (int t = 0; t < NT; ++t) {
    float e[4];
#pragma unroll
    for (int r = 0; r < 4; ++r) { e[r] = __builtin_amdgcn_exp2f(fmaf(sc[t][r], SC_LOG2, -mm)); lsum += e[r]; }
    pb[t] = v2u{pack2(e[0], e[1]), pack2(e[2], e[3])};
  }
  lsum = sum_16_32(lsum);
  if (lane < 8) lds_st<v2u>(L + L_STAT + (w * 8 + lane) * 8, v2u{__float_as_uint(mm), __float_as_uint(lsum)});
  flag_add(L, FL_STAT);

  int va[8];
  {
    const int q = (lane & 15) >> 2, pp = lane & 3, row = 4 * g + q;
#pragma unroll
    for (int u = 0; u < 8; ++u) va[u] = row * 256 + (((2 * u + (pp >> 1)) ^ ((row & 7) << 1)) << 4) + (pp & 1) * 8;
  }
  f32x4 o[8];
#pragma unroll
  for (int u = 0; u < 8; ++u) o[u] = f32x4{0.f, 0.f, 0.f, 0.f};
#pragma unroll
  for (int t = NT; t < 2 * NT; ++t) {
    WAIT_VM(4 * (LI(t - 1) - t));
    v2u vf[8];
#pragma unroll
    for (int u = 0; u < 8; ++u) vf[u] = lds_tr(rb + (t & 7) * 4096 + va[u]);
    WAIT_LGKM0();
    REFILL(t);
#pragma unroll
    for (int u = 0; u < 8; ++u) o[u] = mfma16(vf[u], pb[t - NT], o[u]);
  }
  if (n < 8) {
#pragma unroll
    for (int u = 0; u < 8; ++u) lds_st<f32x4>(rb + 4 * 4096 + n * 528 + (16 * u + 4 * g) * 4, o[u]);
  }
  flag_add(L, FL_ODONE);
#undef TOFF
#undef ISSUE
#undef LI
#undef REFILL
}

__device__ __forceinline__ Tag load_epoch(const Params& p) {
  return Tag{__hip_atomic_load((GAS unsigned*)(p.ws + WS_EPOCH), __ATOMIC_RELAXED, __HIP_MEMORY_SCOPE_AGENT), 0u};
}
__device__ __forceinline__ void make_tag(Tag& t) { PIN(t.a); t.a += 1u; t.b = ~t.a; }
__device__ __forceinline__ void q_offsets(int (&off)[4], int x, int ct) {
#pragma unroll
  for (int k = 0; k < 4; ++k) { off[k] = WS_F2 + ((k * 2 + (x >> 2)) * 1024 + (x & 3) * 256 + ct) * 16; PIN(off[k]); }
}
template <int CAN>
__device__ __forceinline__ void gather_q(lptr L, __amdgpu_buffer_rsrc_t rs, const int (&off)[4], int ct, Tag tg) {
  v4u v[4];
  sweep<4, CAN>(L, rs, off, v, tg);
  const f32x4 s = (gval(v[0]) + gval(v[1])) + (gval(v[2]) + gval(v[3]));
  lds_st<v2u>(L + L_Q + ct * 8, v2u{pack2(s[0], s[1]), pack2(s[2], s[3])});
  wg_barrier();
}
__device__ __forceinline__ void stats_exchange(lptr L, __amdgpu_buffer_rsrc_t rs, int x, int i, int lane, Tag tg) {
  flag_wait<4>(L, FL_STAT, 4);
  const int h = lane & 7;
  float mw[4], lw[4];
#pragma unroll
  for (int w = 0; w < 4; ++w) { const v2u s = lds_ld<v2u>(L + L_STAT + (w * 8 + h) * 8); mw[w] = __uint_as_float(s.x); lw[w] = __uint_as_float(s.y); }
  const float mc = fmaxf(fmaxf(mw[0], mw[1]), fmaxf(mw[2], mw[3]));
  float lc = 0.f;
#pragma unroll
  for (int w = 0; w < 4; ++w) lc += lw[w] * __builtin_amdgcn_exp2f(mw[w] - mc);
  if (lane < 8) put(rs, WS_ST + ((x * 32 + i) * 8 + h) * 16, __float_as_uint(mc), __float_as_uint(lc), tg);
  int off[4]; v4u v[4];
#pragma unroll
  for (int k = 0; k < 4; ++k) off[k] = WS_ST + ((x * 32 + (lane >> 3) + 8 * k) * 8 + h) * 16;
  sweep<4, 16>(L, rs, off, v, tg);
  float M = -INFINITY;
#pragma unroll
  for (int k = 0; k < 4; ++k) M = fmaxf(M, __uint_as_float(v[k].x));
  M = fmaxf(M, dpp<ROR8>(M));
  M = max_16_32(M);
  float S = 0.f;
#pragma unroll
  for (int k = 0; k < 4; ++k) S += __uint_as_float(v[k].z) * __builtin_amdgcn_exp2f(__uint_as_float(v[k].x) - M);
  S += dpp<ROR8>(S);
  S = sum_16_32(S);
  const float inv = 1.0f / S;
  if (lane < 32) {
    const int w = lane >> 3;
    const float mws = w == 0 ? mw[0] : w == 1 ? mw[1] : w == 2 ? mw[2] : mw[3];
    lds_st<float>(L + L_FAC + lane * 4, __builtin_amdgcn_exp2f(mws - M) * inv);
  }
  flag_add(L, FL_FAC);
}
__device__ __forceinline__ void publish_partial(lptr L, __amdgpu_buffer_rsrc_t rs, int x, int i, int ct, Tag tg) {
  const int h = ct >> 5, gi = ct & 31;
  int off = WS_H1 + (((((x >> 2) * 32 + (x & 3) * 8 + h) * 4 + (gi >> 3)) * 32 + i) * 8 + (gi & 7)) * 16;
  PIN(off);
  flag_wait<2>(L, FL_FAC, 1);
  float f[4];
#pragma unroll
  for (int w = 0; w < 4; ++w) { f[w] = lds_ld<float>(L + L_FAC + (w * 8 + h) * 4); PIN(f[w]); }
  flag_wait<4>(L, FL_ODONE, 3);
  flag_wait<0>(L, FL_ODONE, 4);
  f32x4 s = lds_ld<f32x4>(L + L_RING + 4 * 4096 + h * 528 + gi * 16) * f[0];
#pragma unroll
  for (int w = 1; w < 4; ++w) s += lds_ld<f32x4>(L + L_RING + w * 32768 + 4 * 4096 + h * 528 + gi * 16) * f[w];
  put4(rs, off, s, tg);
}

__device__ __forceinline__ void chain_a(const Params& p, lptr L, __amdgpu_buffer_rsrc_t rs, int x, int i, int cw, int lane) {
  const int a = 16 * x + i, ct = cw * 64 + lane, g = lane >> 4;
  const int cg = a >> 2, ks = a & 3, row0 = 256 * ks + 64 * cw;
  v4u W[16];
  const unsigned cpair = *(const GAS unsigned*)(p.c + (lane >> 5) * D + row0 + 2 * (lane & 31));
  __builtin_amdgcn_sched_barrier(0);
#pragma unroll
  for (int u = 0; u < 8; ++u) W[u] = ldw(p.wmod + (size_t)(row0 + 8 * u + (lane >> 3)) * NMOD + 64 * cg + 8 * (lane & 7));
  __builtin_amdgcn_sched_barrier(0);
  Tag tg = load_epoch(p);
  __builtin_amdgcn_sched_barrier(0);
  const Delta dl = make_delta(lane);
  unsigned spair = pack2(silu(bf_lo(cpair)), silu(bf_hi(cpair)));
  PIN(spair);
  {
    int foff = WS_F1 + ((ks * 2 + (g >> 1)) * 512 + 16 * cg + 2 * (lane & 7) + (g & 1)) * 16;
    PIN(foff);
    const int rr = 2 * (lane & 31), b = lane >> 5;
    lds_st<unsigned>(L + L_ACT + cw * 256 + (b * 64 + ((rr >> 1) & 3) * 16 + (rr >> 3) * 2) * 2, spair);
    const int bb = (lane >> 3) & 1;
    const v4u a0 = lds_ld<v4u>(L + L_ACT + cw * 256 + (bb * 64 + g * 16) * 2), a1 = lds_ld<v4u>(L + L_ACT + cw * 256 + (bb * 64 + g * 16) * 2 + 16);
    v4u da[8][2];
#pragma unroll
    for (int u = 0; u < 8; ++u) {
      const unsigned pr = u < 4 ? a0[u] : a1[u - 4];
      da[u][0] = delta_op(dl, pr, 0); da[u][1] = delta_op(dl, pr, 1);
      PIN(da[u][0]); PIN(da[u][1]);
    }
    f32x4 acc0 = {0.f, 0.f, 0.f, 0.f}, acc1 = acc0;
#pragma unroll
    for (int u = 0; u < 8; ++u) {
      acc0 = mfma32(da[u][0], W[u], acc0);
      acc1 = mfma32(da[u][1], W[u], acc1);
    }
    f32x4 t = (lane & 8) ? acc1 : acc0;
#pragma unroll
    for (int e = 0; e < 4; ++e) t[e] += dpp<ROR8>(t[e]);
    if (cw != 0) lds_st<f32x4>(L + L_RED + cw * 1024 + lane * 16, t);
    wg_barrier();
    make_tag(tg);
    if (cw == 0) {
#pragma unroll
      for (int ww = 1; ww < 4; ++ww) t += lds_ld<f32x4>(L + L_RED + ww * 1024 + lane * 16);
      if ((lane & 8) == 0) put4(rs, foff, t, tg);
    }
  }
  v2u xown = *(const GAS v2u*)(p.x + ((lane >> 1) & 1) * D + 8 * a + 4 * (lane & 1));
  v2u b2own = *(const GAS v2u*)(p.b2 + 8 * a + 4 * (lane & 1));
  __builtin_amdgcn_sched_barrier(0);
  const int cgm = cg ^ 1;
  v4u W2[16];
  load_tile128(W2, p.wmod, NMOD, 256 * ks, 2048 + 128 * cgm, cw, lane);
#pragma unroll
  for (int e = 0; e < 16; ++e) W[e] = ldw(rows32_ptr(p.wo, 32 * a, cw, lane, e));
  __builtin_amdgcn_sched_barrier(0);
  PIN(xown); PIN(b2own);
  {
    const int rr = 2 * (lane & 31), bb = lane >> 5;
    lptr ap = L + L_Y + (((bb * 4 + cw) * 4 + (rr & 3)) * 16 + (rr >> 2)) * 2;
    lds_st<u16>(ap, (u16)spair);
    lds_st<u16>(ap + 32, (u16)(spair >> 16));
  }
  { int offq[4]; q_offsets(offq, x, ct); gather_q<4>(L, rs, offq, ct, tg); }
  {
    f32x4 s = gemv_tile128(L + L_Y, W2, dl, cw, lane);
    if (cw != 0) { lds_st<f32x4>(L + L_RED + cw * 1024 + lane * 16, s); flag_add(L, FL_RED); }
    else {
      flag_wait<0>(L, FL_RED, 3);
#pragma unroll
      for (int ww = 1; ww < 4; ++ww) s += lds_ld<f32x4>(L + L_RED + ww * 1024 + lane * 16);
      put4(rs, WS_F3A + ((ks * 2 + (g >> 1)) * 1024 + 32 * cgm + 2 * (lane & 15) + (g & 1)) * 16, s, tg);
    }
  }
  if (cw == 0) {
    const int v = (lane >> 2) & 3, b = (lane >> 1) & 1, half = lane & 1;
    int off[1] = {WS_F3A + (((lane >> 4) * 2 + b) * 1024 + v * 256 + 2 * a + half) * 16};
    v4u gv[1];
    sweep<1, 16>(L, rs, off, gv, tg);
    f32x4 s = gval(gv[0]);
#pragma unroll
    for (int e = 0; e < 4; ++e) s[e] = sum_16_32(s[e]);
    const unsigned lo = pack2(s[0], s[1]), hi = pack2(s[2], s[3]);
    if (lane < 16) {
      if (v == 1 || v == 2) put(rs, WS_F3B + ((b * 2 + v - 1) * 256 + 2 * a + half) * 16, lo, hi, tg);
      else lds_st<f32x4>(L + L_GAB + ((v ? 1 : 0) * 4 + b * 2 + half) * 16, f32x4{bf_lo(lo), bf_hi(lo), bf_lo(hi), bf_hi(hi)});
    }
    stats_exchange(L, rs, x, i, lane, tg);
  }
  int off1[8];
#pragma unroll
  for (int k = 0; k < 8; ++k) { off1[k] = WS_H1 + ((((((lane >> 3) & 1) * 32 + (a >> 2)) * 4 + (a & 3)) * 32 + 4 * k + g) * 8 + (lane & 7)) * 16; PIN(off1[k]); }
  int bp0 = (32 * ((lane >> 3) & 1) + g) * 4, bp1 = bp0 + 64;
  int off2 = WS_H2 + (lane >> 2) * 8192 + a * 64 + (lane & 3) * 16, off4 = off2 + (WS_H4 - WS_H2);
  int put2 = owner_put_off(WS_H2, a, cw, lane);
  PIN(bp0); PIN(bp1); PIN(off2); PIN(off4); PIN(put2);
  v2u sel = sel_op(lane); PIN(sel);
  publish_partial(L, rs, x, i, ct, tg);
  {
    v4u v[8];
    sweep<8, 1>(L, rs, off1, v, tg);
    f32x4 s = {0.f, 0.f, 0.f, 0.f};
#pragma unroll
    for (int k = 0; k < 8; ++k) s = gsum_t(sel, v[k], s);
    const unsigned p01 = pack2(s[0], s[1]), p23 = pack2(s[2], s[3]);
    const v4u av = {(unsigned)__builtin_amdgcn_ds_bpermute(bp0, (int)p01), (unsigned)__builtin_amdgcn_ds_bpermute(bp0, (int)p23),
                    (unsigned)__builtin_amdgcn_ds_bpermute(bp1, (int)p01), (unsigned)__builtin_amdgcn_ds_bpermute(bp1, (int)p23)};
    f32x4 acc0, acc1;
    gemv_rows32(W, dl, av, acc0, acc1);
    put4(rs, put2, acc0, tg);
    put4(rs, put2 + 1024, acc1, tg);
  }
  if (cw != 0) return;
  const f32x4 ga = lds_ld<f32x4>(L + L_GAB + (lane & 3) * 16), gb = lds_ld<f32x4>(L + L_GAB + (4 + (lane & 3)) * 16);
  const f32x4 xv = {bf_lo(xown.x), bf_hi(xown.x), bf_lo(xown.y), bf_hi(xown.y)};
  int off3 = WS_H3 + (((lane >> 2) * 128 + a) * 8 + (lane & 3)) * 16;
  PIN(off3);
  const f32x4 attn = gather_owner<128, 1>(L, rs, off2, sel, tg);
  f32x4 x1;
#pragma unroll
  for (int e = 0; e < 4; ++e) x1[e] = xv[e] + ga[e] * rbf(attn[e]);
  const unsigned x1lo = pack2(x1[0], x1[1]), x1hi = pack2(x1[2], x1[3]);
  if (lane < 4 * NREP3) put(rs, off3, x1lo, x1hi, tg);
  const unsigned long long tpub = __builtin_amdgcn_s_memrealtime();
  const f32x4 x1r = {bf_lo(x1lo), bf_hi(x1lo), bf_lo(x1hi), bf_hi(x1hi)};
  const f32x4 bv = {bf_lo(b2own.x), bf_hi(b2own.x), bf_lo(b2own.y), bf_hi(b2own.y)};
  delay_since(tpub, 150);
  const f32x4 mlp = gather_owner<112, 1>(L, rs, off4, sel, tg);
  if (lane < 4) {
    f32x4 r;
#pragma unroll
    for (int e = 0; e < 4; ++e) r[e] = x1r[e] + gb[e] * (mlp[e] + bv[e]);
    if (*(volatile LAS unsigned*)(L + L_FLAG + FL_ABORT * 4)) r = f32x4{NAN, NAN, NAN, NAN};
    *(GAS v2u*)(p.out + (lane >> 1) * D + 8 * a + 4 * (lane & 1)) = v2u{pack2(r[0], r[1]), pack2(r[2], r[3])};
    if (a == 0 && lane == 0) __hip_atomic_store((GAS unsigned*)(p.ws + WS_EPOCH), tg.a, __ATOMIC_RELAXED, __HIP_MEMORY_SCOPE_AGENT);
  }
}

__device__ __forceinline__ void ln_stats(f32x2 (&v)[8], float& mean, float& rstd) {
  const float shift = __builtin_bit_cast(float, __builtin_amdgcn_readfirstlane(__builtin_bit_cast(int, v[0][0])));
  const f32x2 sh = {shift, shift};
#pragma unroll
  for (int k = 0; k < 8; ++k) v[k] -= sh;
  const f32x2 s2 = ((v[0] + v[1]) + (v[2] + v[3])) + ((v[4] + v[5]) + (v[6] + v[7]));
  f32x2 qa = v[0] * v[0], qb = v[1] * v[1];
#pragma unroll
  for (int k = 2; k < 8; k += 2) { qa = __builtin_elementwise_fma(v[k], v[k], qa); qb = __builtin_elementwise_fma(v[k + 1], v[k + 1], qb); }
  qa += qb;
  float s = s2[0] + s2[1], q = qa[0] + qa[1];
  wave_sum2(s, q);
  mean = s * (1.0f / D);
  rstd = __builtin_amdgcn_rsqf(fmaxf(q * (1.0f / D) - mean * mean, 0.f) + LN_EPS);
}
__device__ __forceinline__ void chain_b(const Params& p, lptr L, __amdgpu_buffer_rsrc_t rs, int x, int i, int cw, int lane) {
  const int j = 16 * x + (i - 16), ct = cw * 64 + lane, g = lane >> 4;
  const int ks = x & 3, cgrp = (x >> 2) * 16 + (i - 16);
  const int b = cw >> 1, gi = 32 * (cw & 1) + (lane & 31);
  v4u W[16];
  const v4u xa = *(const GAS v4u*)(p.x + b * D + 16 * lane), xb = *(const GAS v4u*)(p.x + b * D + 16 * lane + 8);
  unsigned xs = *(const GAS unsigned*)(p.x + b * D + 256 * ks + 4 * gi + 2 * (lane >> 5));
  Tag tg = load_epoch(p);
  __builtin_amdgcn_sched_barrier(0);
  __builtin_amdgcn_s_sleep(12);
  load_tile128(W, p.wq, 4096, 256 * ks, 128 * cgrp, cw, lane);
  unsigned b1u = *(const GAS u16*)(p.b1 + 32 * (j < 112 ? j : 0) + (lane & 31));
  __builtin_amdgcn_sched_barrier(0);
  const Delta dl = make_delta(lane);
  PIN(xs);
  make_tag(tg);
  {
    f32x2 xv[8];
#pragma unroll
    for (int e = 0; e < 4; ++e) { xv[e] = f32x2{bf_lo(xa[e]), bf_hi(xa[e])}; xv[4 + e] = f32x2{bf_lo(xb[e]), bf_hi(xb[e])}; }
    const float shift = __builtin_bit_cast(float, __builtin_amdgcn_readfirstlane(__builtin_bit_cast(int, xv[0][0])));
    float mean, rstd;
    ln_stats(xv, mean, rstd);
    float xn0 = (bf_lo(xs) - shift - mean) * rstd, xn1 = (bf_hi(xs) - shift - mean) * rstd;
    PIN(xn0); PIN(xn1);
    int off[4]; v4u v[4];
#pragma unroll
    for (int k = 0; k < 4; ++k) { off[k] = WS_F1 + ((k * 2 + b) * 512 + (lane >> 5) * 256 + 64 * ks + gi) * 16; PIN(off[k]); }
    int ao = L_ACT + (((b * 4 + (gi >> 4)) * 4 + 2 * (lane >> 5)) * 16 + (gi & 15)) * 2;
    PIN(ao);
    sweep<4, 3>(L, rs, off, v, tg);
    const f32x4 t = (gval(v[0]) + gval(v[1])) + (gval(v[2]) + gval(v[3]));
    const unsigned lo = pack2(t[0], t[1]), hi = pack2(t[2], t[3]);
    const auto slo = __builtin_amdgcn_permlane32_swap(lo, lo, false, false), shi = __builtin_amdgcn_permlane32_swap(hi, hi, false, false);
    const unsigned sa = lane < 32 ? slo[0] : shi[0], ca = lane < 32 ? slo[1] : shi[1];
    const unsigned hp = pack2(xn0 * (1.0f + bf_lo(ca)) + bf_lo(sa), xn1 * (1.0f + bf_hi(ca)) + bf_hi(sa));
    lds_st<u16>(L + ao, (u16)hp);
    lds_st<u16>(L + ao + 32, (u16)(hp >> 16));
  }
  int offq[4];
  q_offsets(offq, x, ct);
  int f2off = WS_F2 + ((ks * 2 + (g >> 1)) * 1024 + 32 * cgrp + 2 * (lane & 15) + (g & 1)) * 16;
  PIN(f2off);
  wg_barrier();
  {
    f32x4 s = gemv_tile128(L + L_ACT, W, dl, cw, lane);
    if (cw != 0) lds_st<f32x4>(L + L_RED + cw * 1024 + lane * 16, s);
    wg_barrier();
    if (cw == 0) {
#pragma unroll
      for (int ww = 1; ww < 4; ++ww) s += lds_ld<f32x4>(L + L_RED + ww * 1024 + lane * 16);
      put4(rs, f2off, s, tg);
    }
  }
  delay_since(__builtin_amdgcn_s_memrealtime(), 50);
  gather_q<0>(L, rs, offq, ct, tg);
  PIN(b1u);
  if (cw == 0) stats_exchange(L, rs, x, i, lane, tg);
  int off3[4];
  if (j < 112) {
    int off[4]; v4u v[4];
#pragma unroll
    for (int k = 0; k < 4; ++k) off[k] = WS_F3B + (k * 256 + ct) * 16;
    sweep<4, 16>(L, rs, off, v, tg);
#pragma unroll
    for (int k = 0; k < 4; ++k) lds_st<v2u>(L + L_SBCB + (k * 1024 + 4 * ct) * 2, v2u{v[k].x, v[k].z});
#pragma unroll
    for (int k = 0; k < 4; ++k) { off3[k] = WS_H3 + ((x * 128 + 32 * k + (lane >> 1)) * 8 + 2 * (cw & 1) + (lane & 1)) * 16; PIN(off3[k]); }
  }
  unsigned voW[4];
#pragma unroll
  for (int v = 0; v < 4; ++v) { voW[v] = (16 * v + (lane >> 2)) * (FF * 2) + (((lane & 3) ^ (((lane >> 4) & 1) << 1)) << 4) - v * 1024; PIN(voW[v]); }
  publish_partial(L, rs, x, i, ct, tg);
  if (j >= 112) return;
  v4u W2[16];
  {
    const char* W1b = (const char*)p.w1 + (size_t)(256 * cw) * (FF * 2) + j * 64;
    const unsigned ring = (unsigned)(size_t)L + L_RING + cw * 32768;
#pragma unroll
    for (int u = 0; u < 4; ++u) {
      const char* b_ = W1b + (size_t)u * 64 * (FF * 2); const unsigned la_ = ring + u * 4096;
      if (u >= PACE) WAIT_VM(4 * (PACE - 1));
      DMA_TILE(voW[0], voW[1], voW[2], voW[3], b_, la_, "");
    }
#pragma unroll
    for (int u = 0; u < 4; ++u) {
      WAIT_VM(4 * (PACE - 1));
#pragma unroll
      for (int v = 0; v < 4; ++v) W2[4 * u + v] = ldw(rows32_ptr(p.w2, 32 * j, cw, lane, 4 * u + v));
      __builtin_amdgcn_sched_barrier(0);
    }
  }
  wg_barrier();
  WAIT_VM(16);
  v4u w1f[8][2];
  {
    const int q = (lane & 15) >> 2, pp = lane & 3;
#pragma unroll
    for (int kk = 0; kk < 8; ++kk)
#pragma unroll
      for (int ch = 0; ch < 2; ++ch) {
        v2u w[2];
#pragma unroll
        for (int hh = 0; hh < 2; ++hh) {
          const int row = (kk & 1) * 32 + 16 * hh + 4 * g + q;
          const int pc = (2 * ch + (pp >> 1)) ^ (((row >> 2) & 1) << 1);
          w[hh] = lds_tr(L + L_RING + cw * 32768 + (kk >> 1) * 4096 + row * 64 + pc * 16 + (pp & 1) * 8);
        }
        w1f[kk][ch] = v4u{w[0].x, w[0].y, w[1].x, w[1].y};
        PIN(w1f[kk][ch]);
      }
  }
  int yrd = L_Y + ((lane & 1) * D + 256 * cw + 8 * g) * 2;
  int redw = L_RED + (cw * 32 + (lane & 15)) * 8, redr = L_RED + (lane & 31) * 8 + (lane >> 5) * 4;
  int hidw = L_HID + cw * 128 + ((lane >> 5) * 32 + (lane & 3) * 8 + ((lane & 31) >> 2)) * 2, hidr = L_HID + cw * 128 + (((lane >> 3) & 1) * 32 + g * 8) * 2;
  int put4o = owner_put_off(WS_H4, j, cw, lane);
  PIN(yrd); PIN(redw); PIN(redr); PIN(hidw); PIN(hidr); PIN(put4o);
  if (cw < 2) {
    f32x2 sb[8], cb[8];
#pragma unroll
    for (int k = 0; k < 4; ++k) {
      const int col = 4 * (64 * k + lane);
      const v2u s = lds_ld<v2u>(L + L_SBCB + ((cw * 2 + 0) * 1024 + col) * 2), c = lds_ld<v2u>(L + L_SBCB + ((cw * 2 + 1) * 1024 + col) * 2);
      sb[2 * k] = f32x2{bf_lo(s.x), bf_hi(s.x)}; sb[2 * k + 1] = f32x2{bf_lo(s.y), bf_hi(s.y)};
      cb[2 * k] = f32x2{1.0f + bf_lo(c.x), 1.0f + bf_hi(c.x)}; cb[2 * k + 1] = f32x2{1.0f + bf_lo(c.y), 1.0f + bf_hi(c.y)};
      PIN(sb[2 * k]); PIN(sb[2 * k + 1]); PIN(cb[2 * k]); PIN(cb[2 * k + 1]);
    }
    int ypos = L_Y + (cw * D + (lane >> 3) * 32 + (lane & 3) * 8 + ((lane >> 2) & 1) * 4) * 2;
    PIN(ypos);
    v4u v[4];
    sweep<4, 0>(L, rs, off3, v, tg);
    f32x2 y[8];
#pragma unroll
    for (int k = 0; k < 4; ++k) { y[2 * k] = f32x2{bf_lo(v[k].x), bf_hi(v[k].x)}; y[2 * k + 1] = f32x2{bf_lo(v[k].z), bf_hi(v[k].z)}; }
    float mean, rstd;
    ln_stats(y, mean, rstd);
    {
      const f32x2 r2 = {rstd, rstd}, m2 = {mean, mean};
#pragma unroll
      for (int k = 0; k < 8; ++k) y[k] = __builtin_elementwise_fma((y[k] - m2) * r2, cb[k], sb[k]);
    }
    ln_stats(y, mean, rstd);
    {
      const f32x2 r2 = {rstd, rstd}, c2 = {-mean * rstd, -mean * rstd};
#pragma unroll
      for (int k = 0; k < 8; ++k) y[k] = __builtin_elementwise_fma(y[k], r2, c2);
    }
#pragma unroll
    for (int k = 0; k < 4; ++k) lds_st<v2u>(L + ypos + k * 512, v2u{pack2(y[2 * k][0], y[2 * k][1]), pack2(y[2 * k + 1][0], y[2 * k + 1][1])});
  }
  wg_barrier();
  {
    f32x4 acc[2] = {{0.f, 0.f, 0.f, 0.f}, {0.f, 0.f, 0.f, 0.f}};
#pragma unroll
    for (int kk = 0; kk < 8; ++kk) {
      const v4u ya = lds_ld<v4u>(L + yrd + 64 * kk);
      acc[0] = mfma32(ya, w1f[kk][0], acc[0]);
      acc[1] = mfma32(ya, w1f[kk][1], acc[1]);
    }
    if (lane < 16) {
#pragma unroll
      for (int ch = 0; ch < 2; ++ch) lds_st<f32x2>(L + redw + ch * 128, f32x2{acc[ch][0], acc[ch][1]});
    }
    wg_barrier();
    float a = bf_lo(b1u);
#pragma unroll
    for (int ww = 0; ww < 4; ++ww) a += lds_ld<float>(L + redr + ww * 256);
    const float hv = gelu_tanh(rbf(a));
    lds_st<u16>(L + hidw, (u16)pack2(hv, hv));
    const v4u av = lds_ld<v4u>(L + hidr);
    f32x4 acc0, acc1;
    gemv_rows32(W2, dl, av, acc0, acc1);
    put4(rs, put4o, acc0, tg);
    put4(rs, put4o + 1024, acc1, tg);
  }
}

__global__ void __launch_bounds__(512, 2) fused_decode_block(Params p) {
  extern __shared__ __attribute__((aligned(16))) unsigned char smem[];
  lptr L = (lptr)smem;
  const int tid = threadIdx.x, lane = tid & 63, wave = __builtin_amdgcn_readfirstlane(tid >> 6);
  const int x = blockIdx.x & 7, ib = blockIdx.x >> 3;
  const int i = (((ib >> 2) & 1) << 4) | ((ib >> 3) << 2) | (ib & 3);
  if (wave < 4) {
    if (tid < 16) { *(LAS unsigned*)(L + L_FLAG + tid * 4) = 0u; WAIT_LGKM0(); }
    if (i < 16) stream_wave<true>(p, L, x, i, wave, lane);
    else stream_wave<false>(p, L, x, i, wave, lane);
    return;
  }
  __builtin_amdgcn_s_setprio(2);
  const __amdgpu_buffer_rsrc_t rs = __builtin_amdgcn_make_buffer_rsrc((void*)p.ws, 0, WS_END, 0x00020000);
  if (i < 16) chain_a(p, L, rs, x, i, wave - 4, lane);
  else chain_b(p, L, rs, x, i, wave - 4, lane);
}

}

extern "C" void kernel_launch(void* const* d_in, const int* in_sizes, int n_in, void* d_out, int out_size, void* d_ws, size_t ws_size,
                              hipStream_t stream) {
  Params p;
  p.x = (const u16*)d_in[0];
  p.K = (const u16*)d_in[1];
  p.V = (const u16*)d_in[2];
  p.wq = (const u16*)d_in[3];
  p.wo = (const u16*)d_in[4];
  p.w1 = (const u16*)d_in[6];
  p.b1 = (const u16*)d_in[7];
  p.w2 = (const u16*)d_in[8];
  p.b2 = (const u16*)d_in[9];
  p.c = (const u16*)d_in[10];
  p.wmod = (const u16*)d_in[11];
  p.out = (u16*)d_out;
  p.ws = (unsigned char*)d_ws;
  (void)hipFuncSetAttribute((const void*)fused_decode_block, hipFuncAttributeMaxDynamicSharedMemorySize, LDS_BYTES);
  hipLaunchKernelGGL(fused_decode_block, dim3(256), dim3(512), LDS_BYTES, stream, p);
}
```
